```python
import torch

def _softmax(x, axis=-1):
    return torch.softmax(x, dim=axis)
def _gelu(x):
    return torch.nn.functional.gelu(x, approximate="tanh")
def _silu(x):
    return torch.nn.functional.silu(x)
def _sigmoid(x):
    return torch.sigmoid(x)
def _layernorm(x, axis=-1, eps=1e-5):
    m = x.mean(dim=axis, keepdim=True)
    v = x.var(dim=axis, keepdim=True, unbiased=False)
    return (x - m) / torch.sqrt(v + eps)
def _scatter_add(tgt, idx, src, axis=0):
    return tgt.clone().index_add_(axis, idx, src)
def _segment_sum(src, seg, n):
    out = torch.zeros((n,) + src.shape[1:], dtype=src.dtype)
    return out.index_add_(0, seg, src)
def _pad(x, pw):
    flat = tuple(v for pair in reversed(pw) for v in pair)
    return torch.nn.functional.pad(x, flat)

kernel_name = 'parallel_sum__spectral_gfnet__dense_activation_mish__prompt'

def setup_inputs():

    _w = (lambda a: (lambda t: t.to(torch.bfloat16) if t.is_floating_point() else t)(torch.as_tensor(a)))
    def _A_setup():
        B, S, D = 1, 2048, 576
        Df = D // 2 + 1
        return {
            "x": _w((lambda *s: torch.randn(*s, dtype=torch.bfloat16) * 0.05)(B, S, D)),
            "w_r": _w((lambda *s: torch.randn(*s, dtype=torch.bfloat16) * 0.8)(S, Df) * 0.02),
            "w_i": _w((lambda *s: torch.randn(*s, dtype=torch.bfloat16) * 0.8)(S, Df) * 0.02),
        }
    _A_in = _A_setup()

    def _B_setup():
        B, S, D, F = 1, 2048, 576, 4608
        return {
            "x": _w((lambda *s: torch.randn(*s, dtype=torch.bfloat16) * 0.05)(B, S, D)),
            "w1": _w((lambda *s: torch.randn(*s, dtype=torch.bfloat16) * 0.8)(D, F) * (D ** -0.5)),
            "b1": _w((lambda *s: torch.randn(*s, dtype=torch.bfloat16) * 0.2)(F)),
            "w2": _w((lambda *s: torch.randn(*s, dtype=torch.bfloat16) * 0.1)(F, D) * (F ** -0.5)),
        }
    _B_in = _B_setup()

    return {
        "A_x": _A_in["x"],
        "A_w_r": _A_in["w_r"],
        "A_w_i": _A_in["w_i"],
        "B_x": _B_in["x"],
        "B_w1": _B_in["w1"],
        "B_b1": _B_in["b1"],
        "B_w2": _B_in["w2"],
    }

def reference(A_x, A_w_r, A_w_i, B_x, B_w1, B_b1, B_w2):
    def _A_ref(x, w_r, w_i):
        B, S, D = x.shape
        X = (lambda x, axes=(-2,-1): torch.fft.rfft2(x.float(), dim=axes))(x, axes=(-2, -1))
        W = w_r + 1j * w_i
        X = X * W
        y = (lambda x, s, axes=(-2,-1): torch.fft.irfft2(x, s=s, dim=axes).to(torch.bfloat16))(X, (S, D), axes=(-2, -1))
        return y

    def _B_ref(x, w1, b1, w2):
        h = torch.einsum("bsd,df->bsf", x, w1) + b1
        sp = torch.log1p(torch.exp(-torch.abs(h))) + torch.where(h > 0, h, torch.zeros_like(h))
        act = h * torch.tanh(sp)
        return torch.einsum("bsf,fd->bsd", act, w2)

    x = A_x
    xn = _layernorm(x, axis=-1)
    return x + _A_ref(xn, A_w_r, A_w_i) + _B_ref(xn, B_w1, B_b1, B_w2)
```

```cpp
#include <hip/hip_runtime.h>
#include <hip/hip_bf16.h>
#if defined(__HIP_DEVICE_COMPILE__)

namespace tb {
struct Cos { double c[576]; };
constexpr double PI = 3.14159265358979323846;
constexpr double cosx(double x) { double t = 1, s = 1; for (int i = 1; i < 14; ++i) { t *= -x * x / ((2 * i - 1) * (2 * i)); s += t; } return s; }
constexpr Cos mkcos() { Cos r{}; for (int i = 0; i < 576; ++i) { int a = i <= 288 ? i : 576 - i; r.c[i] = a < 144 ? cosx(2 * PI * a / 576) : (a == 144 ? 0.0 : -cosx(2 * PI * (288 - a) / 576)); } return r; }
constexpr Cos CT = mkcos();
constexpr unsigned short tobf(double v) { float f = (float)v; unsigned u = __builtin_bit_cast(unsigned, f); return (unsigned short)((u + 0x7fff + ((u >> 16) & 1)) >> 16); }
struct C16 { unsigned short c[576], g1[576], g2[576]; };
constexpr double NRM = 1.0 / (2048.0 * 576.0);
constexpr C16 mk16() { C16 r{}; for (int i = 0; i < 576; ++i) { r.c[i] = tobf(CT.c[i]); r.g1[i] = tobf(NRM * CT.c[i]); r.g2[i] = tobf(2 * NRM * CT.c[i]); } return r; }
constexpr C16 CB = mk16();
constexpr bool kvalid(int j, int kl) { return kl < 36 || (j == 7 && kl == 36); }
constexpr int klof(int c) { return 10 * ((c >> 2) & 3) + 2 * (c >> 4) + ((c >> 1) & 1); }
struct FA { unsigned short v[144 * 64]; };
constexpr FA mkfa(int j, int q) {
  FA r{};
  for (int c = 0; c < 64; c += 2) {
    int kl = klof(c);
    if (!kvalid(j, kl)) continue;
    for (int d = 0; d < 144; ++d) { int n = ((36 * j + kl) * (144 * q + d)) % 576; r.v[d * 64 + c] = CB.c[n]; r.v[d * 64 + c + 1] = CB.c[(n + 144) % 576]; }
  }
  return r;
}
struct FB { unsigned short v[576 * 32]; };
constexpr FB mkfb(int j) {
  FB r{};
  for (int c = 64; c < 80; c += 2) {
    int kl = klof(c);
    if (!kvalid(j, kl)) continue;
    for (int d = 0; d < 576; ++d) { int n = ((36 * j + kl) * d) % 576; r.v[d * 32 + c - 64] = CB.c[n]; r.v[d * 32 + c - 63] = CB.c[(n + 144) % 576]; }
  }
  return r;
}
struct GD { unsigned short v[16 * 576]; };
constexpr GD mkgd(int j, int grp) {
  GD r{};
  for (int p = 0; p < 16; ++p) {
    int c = 16 * grp + 8 * ((p >> 2) & 1) + 4 * (p >> 3) + (p & 3), kl = klof(c), k = 36 * j + kl;
    if (c >= 80 || !kvalid(j, kl)) continue;
    bool one = (k == 0 || k == 288);
    for (int d = 0; d < 576; ++d) { int n = (k * d) % 576; if (c & 1) n = (n + 144) % 576; r.v[p * 576 + d] = one ? CB.g1[n] : CB.g2[n]; }
  }
  return r;
}
struct All { FA fa[32]; FB fb[8]; GD gd[48]; };
#define TB_A(j) constexpr FA a##j##0 = mkfa(j, 0); constexpr FA a##j##1 = mkfa(j, 1); constexpr FA a##j##2 = mkfa(j, 2); constexpr FA a##j##3 = mkfa(j, 3); \
  constexpr FB b##j = mkfb(j); constexpr GD g##j##0 = mkgd(j, 0); constexpr GD g##j##1 = mkgd(j, 1); constexpr GD g##j##2 = mkgd(j, 2); \
  constexpr GD g##j##3 = mkgd(j, 3); constexpr GD g##j##4 = mkgd(j, 4); constexpr GD g##j##5 = mkgd(j, 5);
TB_A(0) TB_A(1) TB_A(2) TB_A(3) TB_A(4) TB_A(5) TB_A(6) TB_A(7)
#define TB_FA(j) a##j##0, a##j##1, a##j##2, a##j##3
#define TB_GD(j) g##j##0, g##j##1, g##j##2, g##j##3, g##j##4, g##j##5
static __device__ const All T = {{TB_FA(0), TB_FA(1), TB_FA(2), TB_FA(3), TB_FA(4), TB_FA(5), TB_FA(6), TB_FA(7)},
                                 {b0, b1, b2, b3, b4, b5, b6, b7},
                                 {TB_GD(0), TB_GD(1), TB_GD(2), TB_GD(3), TB_GD(4), TB_GD(5), TB_GD(6), TB_GD(7)}};
}
#endif

struct KArgs { const void* x; const void* wr; const void* wi; const void* w1; const void* b1; const void* w2; void* out; void* ws; };
__global__ __launch_bounds__(256, 1) void fused_kernel(const void* x, const void* wr, const void* wi, const void* w1, const void* b1, const void* w2, void* out, void* ws) {
#if defined(__HIP_DEVICE_COMPILE__)
  __shared__ char smem[163840];
  unsigned keep = (unsigned)(size_t)smem;
  unsigned bid = blockIdx.x, wv = __builtin_amdgcn_readfirstlane(threadIdx.x >> 6);
  const void* tbl = (const void*)&tb::T;
  asm volatile("; lds base %0" :: "s"(keep));
  asm volatile("s_mov_b64 s[40:41], %[x]\n\ts_and_b32 s41, s41, 0xffff\n\ts_mov_b32 s42, 2359296\n\ts_mov_b32 s43, 0x00020000\n\ts_mov_b32 s97, %[bid]\n\ts_and_b32 s93, s97, 7\n\ts_lshr_b32 s94, s93, 2\n\ts_mul_i32 s95, s94, 16\n\ts_lshr_b32 s93, s97, 3\n\ts_lshr_b32 s94, s93, 1\n\ts_add_u32 s60, s95, s94\n\ts_and_b32 s93, s97, 7\n\ts_and_b32 s95, s93, 3\n\ts_mul_i32 s94, s95, 2\n\ts_lshr_b32 s93, s97, 3\n\ts_and_b32 s95, s93, 1\n\ts_add_u32 s61, s94, s95\n\ts_mov_b32 s62, %[wv]\n\tv_mbcnt_lo_u32_b32 v0, -1, 0\n\tv_mbcnt_hi_u32_b32 v0, -1, v0\n\tv_lshrrev_b32 v188, 3, v0\n\tv_mul_u32_u24 v188, 36864, v188\n\tv_and_b32 v187, 7, v0\n\tv_lshl_add_u32 v188, v187, 4, v188\n\ts_mul_i32 s95, s60, 1152\n\ts_mul_i32 s94, s62, 589824\n\ts_add_u32 s70, s95, s94\n\ts_add_u32 s71, s70, 294912\n\tbuffer_load_dwordx4 v[44:47], v188, s[40:43], s70 offen offset:0\n\tbuffer_load_dwordx4 v[48:51], v188, s[40:43], s70 offen offset:128\n\tbuffer_load_dwordx4 v[52:55], v188, s[40:43], s70 offen offset:256\n\tbuffer_load_dwordx4 v[56:59], v188, s[40:43], s70 offen offset:384\n\tbuffer_load_dwordx4 v[60:63], v188, s[40:43], s70 offen offset:512\n\tbuffer_load_dwordx4 v[64:67], v188, s[40:43], s70 offen offset:640\n\tbuffer_load_dwordx4 v[68:71], v188, s[40:43], s70 offen offset:768\n\tbuffer_load_dwordx4 v[72:75], v188, s[40:43], s70 offen offset:896\n\tbuffer_load_dwordx4 v[76:79], v188, s[40:43], s70 offen offset:1024\n\tbuffer_load_dwordx4 v[80:83], v188, s[40:43], s71 offen offset:0\n\tbuffer_load_dwordx4 v[84:87], v188, s[40:43], s71 offen offset:128\n\tbuffer_load_dwordx4 v[88:91], v188, s[40:43], s71 offen offset:256\n\tbuffer_load_dwordx4 v[92:95], v188, s[40:43], s71 offen offset:384\n\tbuffer_load_dwordx4 v[96:99], v188, s[40:43], s71 offen offset:512\n\tbuffer_load_dwordx4 v[100:103], v188, s[40:43], s71 offen offset:640\n\tbuffer_load_dwordx4 v[104:107], v188, s[40:43], s71 offen offset:768\n\tbuffer_load_dwordx4 v[108:111], v188, s[40:43], s71 offen offset:896\n\tbuffer_load_dwordx4 v[112:115], v188, s[40:43], s71 offen offset:1024\n\ts_mov_b64 s[44:45], %[w1]\n\ts_and_b32 s45, s45, 0xffff\n\ts_mov_b32 s46, 5308416\n\ts_mov_b32 s47, 0x00020000\n\ts_mul_i32 s80, s61, 1152\n\ts_mul_i32 s64, s62, 18432\n\tv_lshrrev_b32 v190, 4, v0\n\tv_and_b32 v192, 3, v0\n\tv_lshrrev_b32 v3, 3, v0\n\ts_mul_i32 s94, s62, 8\n\tv_add_u32 v3, s94, v3\n\tv_bfe_u32 v4, v0, 2, 1\n\tv_and_b32 v187, 1, v190\n\tv_xor_b32 v4, v4, v187\n\tv_lshlrev_b32 v187, 4, v192\n\tv_lshl_add_u32 v4, v4, 6, v187\n\ts_mov_b64 s[24:25], %[tbl]\n\ts_and_b32 s25, s25, 0xffff\n\ts_mov_b32 s26, 1769472\n\ts_mov_b32 s27, 0x00020000\n\ts_mul_i32 s94, s61, 73728\n\ts_add_u32 s84, s94, 0\n\ts_mov_b64 s[48:49], %[w2]\n\ts_and_b32 s49, s49, 0xffff\n\ts_mov_b32 s50, 5308416\n\ts_mov_b32 s51, 0x00020000\n\ts_mul_i32 s81, s61, 663552\n\tv_mul_u32_u24 v8, 128, v3\n\tv_add_u32 v8, v8, v4\n\tv_add_u32 v9, 3072, v8\n\tv_add_u32 v10, 6144, v8\n\ts_add_u32 m0, s64, 0\n\ts_add_u32 s98, s84, 0x0\n\ts_add_u32 s98, s98, s24\n\ts_addc_u32 s99, s25, 0\n\tglobal_load_lds_dwordx4 v8, s[98:99] offset:0\n\tglobal_load_lds_dwordx4 v9, s[98:99] offset:1024\n\tglobal_load_lds_dwordx4 v10, s[98:99] offset:2048\n\ts_add_u32 m0, s64, 3072\n\ts_add_u32 s98, s98, 0x3000\n\ts_addc_u32 s99, s99, 0\n\tglobal_load_lds_dwordx4 v8, s[98:99] offset:0\n\tglobal_load_lds_dwordx4 v9, s[98:99] offset:1024\n\tglobal_load_lds_dwordx4 v10, s[98:99] offset:2048\n\ts_add_u32 m0, s64, 6144\n\ts_add_u32 s98, s98, 0x3000\n\ts_addc_u32 s99, s99, 0\n\tglobal_load_lds_dwordx4 v8, s[98:99] offset:0\n\tglobal_load_lds_dwordx4 v9, s[98:99] offset:1024\n\tglobal_load_lds_dwordx4 v10, s[98:99] offset:2048\n\ts_add_u32 m0, s64, 9216\n\ts_add_u32 s98, s98, 0x3000\n\ts_addc_u32 s99, s99, 0\n\tglobal_load_lds_dwordx4 v8, s[98:99] offset:0\n\tglobal_load_lds_dwordx4 v9, s[98:99] offset:1024\n\tglobal_load_lds_dwordx4 v10, s[98:99] offset:2048\n\ts_mov_b64 s[52:53], %[b1]\n\ts_and_b32 s53, s53, 0xffff\n\ts_mov_b32 s54, 9216\n\ts_mov_b32 s55, 0x00020000\n\ts_mul_i32 s94, s62, 64\n\tv_add_u32 v185, s94, v0\n\tv_lshlrev_b32 v172, 3, v185\n\ts_mul_i32 s70, s61, 1152\n\tbuffer_load_dwordx2 v[186:187], v172, s[52:55], s70 offen\n\ts_getpc_b64 s[82:83]\n\tL_pc_%=:\n\tv_min_u32 v173, 15, v0\n\ts_lshr_b32 s95, s97, 3\n\ts_mul_i32 s94, s95, 16\n\tv_add_u32 v173, s94, v173\n\tv_lshlrev_b32 v173, 7, v173\n\ts_mov_b32 s70, L_end_%=-L_pc_%=-4\n\tv_min_u32 v174, s70, v173\n\tglobal_load_dword v41, v174, s[82:83]\n\ts_mov_b64 s[56:57], %[ws]\n\ts_and_b32 s57, s57, 0xffff\n\ts_mov_b32 s58, 24133888\n\ts_mov_b32 s59, 0x00020000\n\ts_mul_i32 s94, s61, 36864\n\ts_add_u32 s85, s94, 589824\n\ts_mul_i32 s94, s61, 110592\n\ts_add_u32 s86, s94, 884736\n\ts_mov_b32 s69, 0x3f3504f3\n\ts_mov_b32 s76, 0x3f803f80\n\ts_mov_b32 s77, 0xffff0000\n\tv_bfe_u32 v191, v0, 2, 2\n\tv_lshrrev_b32 v193, 5, v0\n\tv_and_b32 v194, 31, v0\n\tv_bfe_u32 v195, v0, 4, 1\n\tv_and_b32 v184, 7, v0\n\tv_xor_b32 v195, v184, v195\n\ts_mul_i32 s94, s62, 4\n\tv_add_u32 v1, s94, v190\n\tv_and_b32 v2, 15, v0\n\tv_lshrrev_b32 v2, 2, v2\n\tv_xor_b32 v2, v2, v190\n\tv_lshlrev_b32 v184, 4, v192\n\tv_lshl_add_u32 v2, v2, 6, v184\n\tv_and_b32 v11, 1, v190\n\tv_lshlrev_b32 v11, 5, v11\n\tv_xor_b32 v184, s62, v191\n\tv_lshl_add_u32 v11, v184, 6, v11\n\tv_lshl_add_u32 v11, v192, 3, v11\n\tv_mul_u32_u24 v184, 36864, v193\n\tv_lshl_add_u32 v184, v191, 8, v184\n\tv_add_u32 v11, v184, v11\n\tv_lshrrev_b32 v12, 1, v191\n\ts_lshr_b32 s94, s62, 1\n\tv_xor_b32 v12, s94, v12\n\tv_and_b32 v184, 1, v190\n\tv_lshlrev_b32 v184, 5, v184\n\tv_lshl_add_u32 v12, v12, 6, v184\n\tv_lshl_add_u32 v12, v192, 3, v12\n\tv_mul_u32_u24 v184, 18432, v193\n\tv_lshl_add_u32 v184, v191, 7, v184\n\tv_add_u32 v12, v184, v12\n\tv_lshrrev_b32 v17, 1, v190\n\tv_lshl_add_u32 v17, v17, 3, v191\n\tv_and_b32 v184, 1, v190\n\ts_lshr_b32 s94, s62, 1\n\tv_xor_b32 v184, s94, v184\n\tv_lshlrev_b32 v184, 5, v184\n\tv_lshl_add_u32 v17, v17, 6, v184\n\tv_lshl_add_u32 v17, v192, 3, v17\n\tv_add_u32 v18, 36864, v17\n\tv_lshrrev_b32 v184, 3, v194\n\tv_mul_u32_u24 v184, 9216, v184\n\tv_and_b32 v183, 7, v0\n\tv_lshl_add_u32 v184, v183, 7, v184\n\tv_add_u32 v184, 73728, v184\n\tv_add_u32 v25, 0, v193\n\tv_xor_b32 v25, v25, v195\n\tv_lshl_add_u32 v25, v25, 4, v184\n\ts_and_b32 s93, s62, 1\n\ts_xor_b32 s95, s93, 0\n\ts_mul_i32 s94, s95, 36864\n\tv_add_u32 v25, s94, v25\n\tv_add_u32 v26, 2, v193\n\tv_xor_b32 v26, v26, v195\n\tv_lshl_add_u32 v26, v26, 4, v184\n\ts_and_b32 s93, s62, 1\n\ts_xor_b32 s95, s93, 0\n\ts_mul_i32 s94, s95, 36864\n\tv_add_u32 v26, s94, v26\n\tv_add_u32 v27, 4, v193\n\tv_xor_b32 v27, v27, v195\n\tv_lshl_add_u32 v27, v27, 4, v184\n\ts_and_b32 s93, s62, 1\n\ts_xor_b32 s95, s93, 0\n\ts_mul_i32 s94, s95, 36864\n\tv_add_u32 v27, s94, v27\n\tv_add_u32 v28, 6, v193\n\tv_xor_b32 v28, v28, v195\n\tv_lshl_add_u32 v28, v28, 4, v184\n\ts_and_b32 s93, s62, 1\n\ts_xor_b32 s95, s93, 0\n\ts_mul_i32 s94, s95, 36864\n\tv_add_u32 v28, s94, v28\n\ts_and_b32 s93, s62, 1\n\ts_mul_i32 s95, s93, 4\n\ts_add_u32 s94, s95, 0\n\tv_add_u32 v19, s94, v193\n\tv_xor_b32 v19, v19, v195\n\tv_lshl_add_u32 v19, v19, 4, v184\n\ts_lshr_b32 s93, s62, 1\n\ts_mul_i32 s95, s93, 1024\n\ts_and_b32 s91, s62, 1\n\ts_xor_b32 s92, s91, 0\n\ts_mul_i32 s93, s92, 36864\n\ts_add_u32 s94, s95, s93\n\tv_add_u32 v19, s94, v19\n\ts_and_b32 s95, s62, 1\n\ts_mul_i32 s93, s95, 4\n\ts_add_u32 s94, s93, 2\n\tv_add_u32 v20, s94, v193\n\tv_xor_b32 v20, v20, v195\n\tv_lshl_add_u32 v20, v20, 4, v184\n\ts_lshr_b32 s95, s62, 1\n\ts_mul_i32 s93, s95, 1024\n\ts_and_b32 s91, s62, 1\n\ts_xor_b32 s92, s91, 0\n\ts_mul_i32 s95, s92, 36864\n\ts_add_u32 s94, s93, s95\n\tv_add_u32 v20, s94, v20\n\tv_add_u32 v29, 0, v193\n\tv_xor_b32 v29, v29, v195\n\tv_lshl_add_u32 v29, v29, 4, v184\n\ts_and_b32 s93, s62, 1\n\ts_xor_b32 s95, s93, 1\n\ts_mul_i32 s94, s95, 36864\n\tv_add_u32 v29, s94, v29\n\tv_add_u32 v30, 2, v193\n\tv_xor_b32 v30, v30, v195\n\tv_lshl_add_u32 v30, v30, 4, v184\n\ts_and_b32 s93, s62, 1\n\ts_xor_b32 s95, s93, 1\n\ts_mul_i32 s94, s95, 36864\n\tv_add_u32 v30, s94, v30\n\tv_add_u32 v31, 4, v193\n\tv_xor_b32 v31, v31, v195\n\tv_lshl_add_u32 v31, v31, 4, v184\n\ts_and_b32 s93, s62, 1\n\ts_xor_b32 s95, s93, 1\n\ts_mul_i32 s94, s95, 36864\n\tv_add_u32 v31, s94, v31\n\tv_add_u32 v32, 6, v193\n\tv_xor_b32 v32, v32, v195\n\tv_lshl_add_u32 v32, v32, 4, v184\n\ts_and_b32 s93, s62, 1\n\ts_xor_b32 s95, s93, 1\n\ts_mul_i32 s94, s95, 36864\n\tv_add_u32 v32, s94, v32\n\ts_and_b32 s93, s62, 1\n\ts_mul_i32 s95, s93, 4\n\ts_add_u32 s94, s95, 0\n\tv_add_u32 v21, s94, v193\n\tv_xor_b32 v21, v21, v195\n\tv_lshl_add_u32 v21, v21, 4, v184\n\ts_lshr_b32 s93, s62, 1\n\ts_mul_i32 s95, s93, 1024\n\ts_and_b32 s91, s62, 1\n\ts_xor_b32 s92, s91, 1\n\ts_mul_i32 s93, s92, 36864\n\ts_add_u32 s94, s95, s93\n\tv_add_u32 v21, s94, v21\n\ts_and_b32 s95, s62, 1\n\ts_mul_i32 s93, s95, 4\n\ts_add_u32 s94, s93, 2\n\tv_add_u32 v22, s94, v193\n\tv_xor_b32 v22, v22, v195\n\tv_lshl_add_u32 v22, v22, 4, v184\n\ts_lshr_b32 s95, s62, 1\n\ts_mul_i32 s93, s95, 1024\n\ts_and_b32 s91, s62, 1\n\ts_xor_b32 s92, s91, 1\n\ts_mul_i32 s95, s92, 36864\n\ts_add_u32 s94, s93, s95\n\tv_add_u32 v22, s94, v22\n\ts_lshr_b32 s93, s62, 1\n\ts_mul_i32 s95, s93, 4\n\ts_add_u32 s94, s95, 0\n\tv_add_u32 v23, s94, v193\n\tv_xor_b32 v23, v23, v195\n\tv_lshl_add_u32 v23, v23, 4, v184\n\ts_and_b32 s93, s62, 1\n\ts_mul_i32 s95, s93, 36864\n\ts_add_u32 s94, s95, 8192\n\tv_add_u32 v23, s94, v23\n\ts_lshr_b32 s93, s62, 1\n\ts_mul_i32 s95, s93, 4\n\ts_add_u32 s94, s95, 2\n\tv_add_u32 v24, s94, v193\n\tv_xor_b32 v24, v24, v195\n\tv_lshl_add_u32 v24, v24, 4, v184\n\ts_and_b32 s93, s62, 1\n\ts_mul_i32 s95, s93, 36864\n\ts_add_u32 s94, s95, 8192\n\tv_add_u32 v24, s94, v24\n\ts_mul_i32 s95, s62, 128\n\ts_add_u32 s94, s95, 147456\n\tv_lshl_add_u32 v33, v193, 4, s94\n\ts_lshr_b32 s93, s62, 1\n\ts_mul_i32 s95, s93, 128\n\ts_add_u32 s94, s95, 149504\n\tv_lshl_add_u32 v34, v193, 4, s94\n\ts_mul_i32 s93, s60, 589824\n\ts_mul_i32 s92, s61, 9216\n\ts_add_u32 s95, s93, s92\n\ts_and_b32 s91, s62, 1\n\ts_xor_b32 s93, s91, 0\n\ts_mul_i32 s92, s93, 294912\n\ts_add_u32 s94, s95, s92\n\ts_add_u32 s65, s94, 0\n\ts_mul_i32 s95, s60, 589824\n\ts_mul_i32 s93, s61, 9216\n\ts_add_u32 s92, s95, s93\n\ts_and_b32 s91, s62, 1\n\ts_xor_b32 s95, s91, 1\n\ts_mul_i32 s93, s95, 294912\n\ts_add_u32 s94, s92, s93\n\ts_add_u32 s66, s94, 0\n\tv_and_b32 v188, 7, v0\n\tv_lshrrev_b32 v184, 3, v0\n\tv_xor_b32 v188, v188, v184\n\ts_and_b32 s94, s62, 1\n\tv_xor_b32 v188, s94, v188\n\tv_lshlrev_b32 v188, 4, v188\n\tv_lshrrev_b32 v184, 3, v0\n\tv_lshl_add_u32 v188, v184, 7, v188\n\ts_mul_i32 s95, s62, 2\n\ts_add_u32 s92, s95, 0\n\ts_mul_i32 s93, s92, 9216\n\ts_add_u32 s94, s93, 73728\n\tv_add_u32 v188, s94, v188\n\tv_and_b32 v189, 7, v0\n\tv_lshrrev_b32 v184, 3, v0\n\tv_xor_b32 v189, v189, v184\n\ts_and_b32 s94, s62, 1\n\tv_xor_b32 v189, s94, v189\n\tv_lshlrev_b32 v189, 4, v189\n\tv_lshrrev_b32 v184, 3, v0\n\tv_lshl_add_u32 v189, v184, 7, v189\n\ts_mul_i32 s95, s62, 2\n\ts_add_u32 s92, s95, 1\n\ts_mul_i32 s93, s92, 9216\n\ts_add_u32 s94, s93, 73728\n\tv_add_u32 v189, s94, v189\n\tv_mov_b32 v43, 0\n\ts_mov_b32 s70, 24133632\n\tbuffer_load_dwordx2 v[220:221], v43, s[56:59], s70 offen sc1\n\ts_mul_i32 s94, s62, 64\n\tv_add_u32 v42, s94, v0\n\tv_cmp_eq_u32 vcc, 0, v42\n\tv_mov_b32 v42, 2147483392\n\ts_nop 0\n\tv_cndmask_b32 v42, v42, v43, vcc\n\tv_lshrrev_b32 v116, 4, v0\n\tv_lshl_add_u32 v116, v116, 8, s60\n\tv_and_b32 v121, 15, v0\n\ts_mul_i32 s95, s62, 16\n\tv_add_u32 v121, s95, v121\n\tv_mul_u32_u24 v116, v116, v121\n\tv_and_b32 v117, 15, v0\n\ts_mul_i32 s95, s62, 16\n\tv_add_u32 v117, s95, v117\n\tv_lshlrev_b32 v117, 5, v117\n\tv_cvt_f32_u32 v116, v116\n\tv_cvt_f32_u32 v117, v117\n\tv_mul_f32 v116, 0xba000000, v116\n\tv_mul_f32 v117, 0xba000000, v117\n\tv_fract_f32 v118, v117\n\tv_cos_f32 v118, v118\n\tv_nop\n\tv_add_f32 v118, v118, v118\n\tv_fract_f32 v122, v116\n\tv_add_f32 v123, v117, v116\n\tv_fract_f32 v123, v123\n\tv_cos_f32 v119, v122\n\tv_cos_f32 v120, v123\n\tv_sin_f32 v122, v122\n\tv_sin_f32 v123, v123\n\tv_cvt_pk_bf16_f32 v196, v119, v120\n\tv_cvt_pk_bf16_f32 v204, v122, v123\n\tv_fma_f32 v121, v118, v120, -v119\n\tv_fma_f32 v124, v118, v123, -v122\n\tv_fma_f32 v119, v118, v121, -v120\n\tv_fma_f32 v122, v118, v124, -v123\n\tv_cvt_pk_bf16_f32 v197, v121, v119\n\tv_cvt_pk_bf16_f32 v205, v124, v122\n\tv_fma_f32 v120, v118, v119, -v121\n\tv_fma_f32 v123, v118, v122, -v124\n\tv_fma_f32 v121, v118, v120, -v119\n\tv_fma_f32 v124, v118, v123, -v122\n\tv_cvt_pk_bf16_f32 v198, v120, v121\n\tv_cvt_pk_bf16_f32 v206, v123, v124\n\tv_fma_f32 v119, v118, v121, -v120\n\tv_fma_f32 v122, v118, v124, -v123\n\tv_fma_f32 v120, v118, v119, -v121\n\tv_fma_f32 v123, v118, v122, -v124\n\tv_cvt_pk_bf16_f32 v199, v119, v120\n\tv_cvt_pk_bf16_f32 v207, v122, v123\n\ts_mov_b32 s90, 0x42000000\n\tv_fma_f32 v122, v117, s90, v116\n\tv_fract_f32 v122, v122\n\ts_mov_b32 s90, 0x42040000\n\tv_fma_f32 v123, v117, s90, v116\n\tv_fract_f32 v123, v123\n\tv_cos_f32 v119, v122\n\tv_cos_f32 v120, v123\n\tv_sin_f32 v122, v122\n\tv_sin_f32 v123, v123\n\tv_cvt_pk_bf16_f32 v200, v119, v120\n\tv_cvt_pk_bf16_f32 v208, v122, v123\n\tv_fma_f32 v121, v118, v120, -v119\n\tv_fma_f32 v124, v118, v123, -v122\n\tv_fma_f32 v119, v118, v121, -v120\n\tv_fma_f32 v122, v118, v124, -v123\n\tv_cvt_pk_bf16_f32 v201, v121, v119\n\tv_cvt_pk_bf16_f32 v209, v124, v122\n\tv_fma_f32 v120, v118, v119, -v121\n\tv_fma_f32 v123, v118, v122, -v124\n\tv_fma_f32 v121, v118, v120, -v119\n\tv_fma_f32 v124, v118, v123, -v122\n\tv_cvt_pk_bf16_f32 v202, v120, v121\n\tv_cvt_pk_bf16_f32 v210, v123, v124\n\tv_fma_f32 v119, v118, v121, -v120\n\tv_fma_f32 v122, v118, v124, -v123\n\tv_fma_f32 v120, v118, v119, -v121\n\tv_fma_f32 v123, v118, v122, -v124\n\tv_cvt_pk_bf16_f32 v203, v119, v120\n\tv_cvt_pk_bf16_f32 v211, v122, v123\n\tv_and_b32 v116, 15, v0\n\ts_and_b32 s94, s62, 1\n\ts_mul_i32 s95, s94, 16\n\tv_add_u32 v116, s95, v116\n\tv_lshrrev_b32 v121, 4, v0\n\tv_lshlrev_b32 v121, 3, v121\n\tv_mul_u32_u24 v116, v116, v121\n\tv_and_b32 v117, 15, v0\n\ts_and_b32 s94, s62, 1\n\ts_mul_i32 s95, s94, 16\n\tv_add_u32 v117, s95, v117\n\tv_cvt_f32_u32 v116, v116\n\tv_cvt_f32_u32 v117, v117\n\tv_mul_f32 v116, 0xbd000000, v116\n\tv_mul_f32 v117, 0xbd000000, v117\n\tv_fract_f32 v118, v117\n\tv_cos_f32 v118, v118\n\tv_nop\n\tv_add_f32 v118, v118, v118\n\tv_fract_f32 v122, v116\n\tv_add_f32 v123, v117, v116\n\tv_fract_f32 v123, v123\n\tv_cos_f32 v119, v122\n\tv_cos_f32 v120, v123\n\tv_sin_f32 v122, v122\n\tv_sin_f32 v123, v123\n\tv_cvt_pk_bf16_f32 v212, v119, v120\n\tv_cvt_pk_bf16_f32 v216, v122, v123\n\tv_fma_f32 v121, v118, v120, -v119\n\tv_fma_f32 v124, v118, v123, -v122\n\tv_fma_f32 v119, v118, v121, -v120\n\tv_fma_f32 v122, v118, v124, -v123\n\tv_cvt_pk_bf16_f32 v213, v121, v119\n\tv_cvt_pk_bf16_f32 v217, v124, v122\n\tv_fma_f32 v120, v118, v119, -v121\n\tv_fma_f32 v123, v118, v122, -v124\n\tv_fma_f32 v121, v118, v120, -v119\n\tv_fma_f32 v124, v118, v123, -v122\n\tv_cvt_pk_bf16_f32 v214, v120, v121\n\tv_cvt_pk_bf16_f32 v218, v123, v124\n\tv_fma_f32 v119, v118, v121, -v120\n\tv_fma_f32 v122, v118, v124, -v123\n\tv_fma_f32 v120, v118, v119, -v121\n\tv_fma_f32 v123, v118, v122, -v124\n\tv_cvt_pk_bf16_f32 v215, v119, v120\n\tv_cvt_pk_bf16_f32 v219, v122, v123\n\ts_mov_b64 s[28:29], %[wr]\n\ts_and_b32 s29, s29, 0xffff\n\ts_mov_b32 s30, 1183744\n\ts_mov_b32 s31, 0x00020000\n\ts_mov_b64 s[36:37], %[wi]\n\ts_and_b32 s37, s37, 0xffff\n\ts_mov_b32 s38, 1183744\n\ts_mov_b32 s39, 0x00020000\n\ts_mov_b32 s87, 0x3f800000\n\ts_mov_b32 s67, 0x3f317218\n\ts_mul_i32 s95, s62, 64\n\tv_add_u32 v172, s95, v0\n\tv_mov_b32 v173, 0x7fffff00\n\ts_lshr_b32 s94, s97, 4\n\ts_mul_i32 s95, s94, 36\n\tv_add_u32 v174, s95, v172\n\tv_lshl_add_u32 v174, v174, 7, s84\n\tv_subrev_u32 v174, 0, v174\n\tv_cmp_le_u32 vcc, 0, v172\n\ts_nop 1\n\tv_cndmask_b32 v173, v173, v174, vcc\n\ts_lshr_b32 s94, s97, 4\n\ts_mul_i32 s95, s94, 18\n\tv_add_u32 v174, s95, v172\n\tv_lshl_add_u32 v174, v174, 7, s85\n\tv_subrev_u32 v174, 4608, v174\n\tv_cmp_le_u32 vcc, 36, v172\n\ts_nop 1\n\tv_cndmask_b32 v173, v173, v174, vcc\n\ts_lshr_b32 s94, s97, 4\n\ts_mul_i32 s95, s94, 54\n\tv_add_u32 v174, s95, v172\n\tv_lshl_add_u32 v174, v174, 7, s86\n\tv_subrev_u32 v174, 6912, v174\n\tv_cmp_le_u32 vcc, 54, v172\n\ts_nop 1\n\tv_cndmask_b32 v173, v173, v174, vcc\n\tv_cmp_gt_u32 vcc, 108, v172\n\tv_mov_b32 v174, 0x7fffff00\n\ts_nop 0\n\tv_cndmask_b32 v173, v174, v173, vcc\n\tbuffer_load_dword v41, v173, s[24:27], 0 offen \n\ts_mul_i32 s95, s62, 64\n\tv_add_u32 v172, s95, v0\n\tv_add_u32 v172, 0, v172\n\tv_cmp_gt_u32 vcc, 324, v172\n\ts_lshr_b32 s94, s97, 4\n\ts_mul_i32 s95, s94, 324\n\tv_add_u32 v172, s95, v172\n\tv_mul_u32_u24 v173, 7282, v172\n\tv_lshrrev_b32 v173, 16, v173\n\tv_mul_u32_u24 v175, 9, v173\n\tv_sub_u32 v172, v172, v175\n\tv_mul_u32_u24 v175, 9216, v173\n\tv_lshl_add_u32 v172, v172, 7, v175\n\tv_mov_b32 v173, 0x7fffff00\n\tv_cndmask_b32 v172, v173, v172, vcc\n\tbuffer_load_dword v41, v172, s[44:47], s80 offen \n\ts_mul_i32 s95, s62, 64\n\tv_add_u32 v172, s95, v0\n\tv_add_u32 v172, 256, v172\n\tv_cmp_gt_u32 vcc, 324, v172\n\ts_lshr_b32 s94, s97, 4\n\ts_mul_i32 s95, s94, 324\n\tv_add_u32 v172, s95, v172\n\tv_mul_u32_u24 v173, 7282, v172\n\tv_lshrrev_b32 v173, 16, v173\n\tv_mul_u32_u24 v175, 9, v173\n\tv_sub_u32 v172, v172, v175\n\tv_mul_u32_u24 v175, 9216, v173\n\tv_lshl_add_u32 v172, v172, 7, v175\n\tv_mov_b32 v173, 0x7fffff00\n\tv_cndmask_b32 v172, v173, v172, vcc\n\tbuffer_load_dword v41, v172, s[44:47], s80 offen \n\tv_and_b32 v176, 15, v0\n\ts_and_b32 s94, s62, 1\n\ts_mul_i32 s95, s94, 16\n\tv_add_u32 v176, s95, v176\n\ts_mul_i32 s94, s60, 2\n\ts_lshr_b32 s93, s62, 1\n\ts_add_u32 s95, s94, s93\n\tv_lshl_add_u32 v176, v176, 6, s95\n\tv_mul_u32_u24 v176, 289, v176\n\ts_mul_i32 s95, s61, 36\n\tv_add_u32 v176, s95, v176\n\tv_lshrrev_b32 v179, 4, v0\n\tv_mul_u32_u24 v179, 10, v179\n\tv_add_u32 v176, v176, v179\n\tv_lshlrev_b32 v176, 1, v176\n\tbuffer_load_dword v41, v176, s[28:31], 0 offen offset:8 \n\tbuffer_load_dword v41, v176, s[36:39], 0 offen offset:8 \n\tv_mov_b32 v124, 0\n\tv_mov_b32 v125, 0\n\tv_mov_b32 v126, 0\n\tv_mov_b32 v127, 0\n\tv_mov_b32 v128, 0\n\tv_mov_b32 v129, 0\n\tv_mov_b32 v130, 0\n\tv_mov_b32 v131, 0\n\ts_waitcnt vmcnt(37)\n\tv_dot2_f32_bf16 v124, v44, s76, v124\n\tv_dot2_f32_bf16 v126, v44, v44, v126\n\tv_dot2_f32_bf16 v125, v45, s76, v125\n\tv_dot2_f32_bf16 v127, v45, v45, v127\n\tv_dot2_f32_bf16 v124, v46, s76, v124\n\tv_dot2_f32_bf16 v126, v46, v46, v126\n\tv_dot2_f32_bf16 v125, v47, s76, v125\n\tv_dot2_f32_bf16 v127, v47, v47, v127\n\ts_waitcnt vmcnt(36)\n\tv_dot2_f32_bf16 v124, v48, s76, v124\n\tv_dot2_f32_bf16 v126, v48, v48, v126\n\tv_dot2_f32_bf16 v125, v49, s76, v125\n\tv_dot2_f32_bf16 v127, v49, v49, v127\n\tv_dot2_f32_bf16 v124, v50, s76, v124\n\tv_dot2_f32_bf16 v126, v50, v50, v126\n\tv_dot2_f32_bf16 v125, v51, s76, v125\n\tv_dot2_f32_bf16 v127, v51, v51, v127\n\ts_waitcnt vmcnt(35)\n\tv_dot2_f32_bf16 v124, v52, s76, v124\n\tv_dot2_f32_bf16 v126, v52, v52, v126\n\tv_dot2_f32_bf16 v125, v53, s76, v125\n\tv_dot2_f32_bf16 v127, v53, v53, v127\n\tv_dot2_f32_bf16 v124, v54, s76, v124\n\tv_dot2_f32_bf16 v126, v54, v54, v126\n\tv_dot2_f32_bf16 v125, v55, s76, v125\n\tv_dot2_f32_bf16 v127, v55, v55, v127\n\ts_waitcnt vmcnt(34)\n\tv_dot2_f32_bf16 v124, v56, s76, v124\n\tv_dot2_f32_bf16 v126, v56, v56, v126\n\tv_dot2_f32_bf16 v125, v57, s76, v125\n\tv_dot2_f32_bf16 v127, v57, v57, v127\n\tv_dot2_f32_bf16 v124, v58, s76, v124\n\tv_dot2_f32_bf16 v126, v58, v58, v126\n\tv_dot2_f32_bf16 v125, v59, s76, v125\n\tv_dot2_f32_bf16 v127, v59, v59, v127\n\ts_waitcnt vmcnt(33)\n\tv_dot2_f32_bf16 v124, v60, s76, v124\n\tv_dot2_f32_bf16 v126, v60, v60, v126\n\tv_dot2_f32_bf16 v125, v61, s76, v125\n\tv_dot2_f32_bf16 v127, v61, v61, v127\n\tv_dot2_f32_bf16 v124, v62, s76, v124\n\tv_dot2_f32_bf16 v126, v62, v62, v126\n\tv_dot2_f32_bf16 v125, v63, s76, v125\n\tv_dot2_f32_bf16 v127, v63, v63, v127\n\ts_waitcnt vmcnt(32)\n\tv_dot2_f32_bf16 v124, v64, s76, v124\n\tv_dot2_f32_bf16 v126, v64, v64, v126\n\tv_dot2_f32_bf16 v125, v65, s76, v125\n\tv_dot2_f32_bf16 v127, v65, v65, v127\n\tv_dot2_f32_bf16 v124, v66, s76, v124\n\tv_dot2_f32_bf16 v126, v66, v66, v126\n\tv_dot2_f32_bf16 v125, v67, s76, v125\n\tv_dot2_f32_bf16 v127, v67, v67, v127\n\ts_waitcnt vmcnt(31)\n\tv_dot2_f32_bf16 v124, v68, s76, v124\n\tv_dot2_f32_bf16 v126, v68, v68, v126\n\tv_dot2_f32_bf16 v125, v69, s76, v125\n\tv_dot2_f32_bf16 v127, v69, v69, v127\n\tv_dot2_f32_bf16 v124, v70, s76, v124\n\tv_dot2_f32_bf16 v126, v70, v70, v126\n\tv_dot2_f32_bf16 v125, v71, s76, v125\n\tv_dot2_f32_bf16 v127, v71, v71, v127\n\ts_waitcnt vmcnt(30)\n\tv_dot2_f32_bf16 v124, v72, s76, v124\n\tv_dot2_f32_bf16 v126, v72, v72, v126\n\tv_dot2_f32_bf16 v125, v73, s76, v125\n\tv_dot2_f32_bf16 v127, v73, v73, v127\n\tv_dot2_f32_bf16 v124, v74, s76, v124\n\tv_dot2_f32_bf16 v126, v74, v74, v126\n\tv_dot2_f32_bf16 v125, v75, s76, v125\n\tv_dot2_f32_bf16 v127, v75, v75, v127\n\ts_waitcnt vmcnt(29)\n\tv_dot2_f32_bf16 v124, v76, s76, v124\n\tv_dot2_f32_bf16 v126, v76, v76, v126\n\tv_dot2_f32_bf16 v125, v77, s76, v125\n\tv_dot2_f32_bf16 v127, v77, v77, v127\n\tv_dot2_f32_bf16 v124, v78, s76, v124\n\tv_dot2_f32_bf16 v126, v78, v78, v126\n\tv_dot2_f32_bf16 v125, v79, s76, v125\n\tv_dot2_f32_bf16 v127, v79, v79, v127\n\ts_waitcnt vmcnt(28)\n\tv_dot2_f32_bf16 v128, v80, s76, v128\n\tv_dot2_f32_bf16 v130, v80, v80, v130\n\tv_dot2_f32_bf16 v129, v81, s76, v129\n\tv_dot2_f32_bf16 v131, v81, v81, v131\n\tv_dot2_f32_bf16 v128, v82, s76, v128\n\tv_dot2_f32_bf16 v130, v82, v82, v130\n\tv_dot2_f32_bf16 v129, v83, s76, v129\n\tv_dot2_f32_bf16 v131, v83, v83, v131\n\ts_waitcnt vmcnt(27)\n\tv_dot2_f32_bf16 v128, v84, s76, v128\n\tv_dot2_f32_bf16 v130, v84, v84, v130\n\tv_dot2_f32_bf16 v129, v85, s76, v129\n\tv_dot2_f32_bf16 v131, v85, v85, v131\n\tv_dot2_f32_bf16 v128, v86, s76, v128\n\tv_dot2_f32_bf16 v130, v86, v86, v130\n\tv_dot2_f32_bf16 v129, v87, s76, v129\n\tv_dot2_f32_bf16 v131, v87, v87, v131\n\ts_waitcnt vmcnt(26)\n\tv_dot2_f32_bf16 v128, v88, s76, v128\n\tv_dot2_f32_bf16 v130, v88, v88, v130\n\tv_dot2_f32_bf16 v129, v89, s76, v129\n\tv_dot2_f32_bf16 v131, v89, v89, v131\n\tv_dot2_f32_bf16 v128, v90, s76, v128\n\tv_dot2_f32_bf16 v130, v90, v90, v130\n\tv_dot2_f32_bf16 v129, v91, s76, v129\n\tv_dot2_f32_bf16 v131, v91, v91, v131\n\ts_waitcnt vmcnt(25)\n\tv_dot2_f32_bf16 v128, v92, s76, v128\n\tv_dot2_f32_bf16 v130, v92, v92, v130\n\tv_dot2_f32_bf16 v129, v93, s76, v129\n\tv_dot2_f32_bf16 v131, v93, v93, v131\n\tv_dot2_f32_bf16 v128, v94, s76, v128\n\tv_dot2_f32_bf16 v130, v94, v94, v130\n\tv_dot2_f32_bf16 v129, v95, s76, v129\n\tv_dot2_f32_bf16 v131, v95, v95, v131\n\ts_waitcnt vmcnt(24)\n\tv_dot2_f32_bf16 v128, v96, s76, v128\n\tv_dot2_f32_bf16 v130, v96, v96, v130\n\tv_dot2_f32_bf16 v129, v97, s76, v129\n\tv_dot2_f32_bf16 v131, v97, v97, v131\n\tv_dot2_f32_bf16 v128, v98, s76, v128\n\tv_dot2_f32_bf16 v130, v98, v98, v130\n\tv_dot2_f32_bf16 v129, v99, s76, v129\n\tv_dot2_f32_bf16 v131, v99, v99, v131\n\ts_waitcnt vmcnt(23)\n\tv_dot2_f32_bf16 v128, v100, s76, v128\n\tv_dot2_f32_bf16 v130, v100, v100, v130\n\tv_dot2_f32_bf16 v129, v101, s76, v129\n\tv_dot2_f32_bf16 v131, v101, v101, v131\n\tv_dot2_f32_bf16 v128, v102, s76, v128\n\tv_dot2_f32_bf16 v130, v102, v102, v130\n\tv_dot2_f32_bf16 v129, v103, s76, v129\n\tv_dot2_f32_bf16 v131, v103, v103, v131\n\ts_waitcnt vmcnt(22)\n\tv_dot2_f32_bf16 v128, v104, s76, v128\n\tv_dot2_f32_bf16 v130, v104, v104, v130\n\tv_dot2_f32_bf16 v129, v105, s76, v129\n\tv_dot2_f32_bf16 v131, v105, v105, v131\n\tv_dot2_f32_bf16 v128, v106, s76, v128\n\tv_dot2_f32_bf16 v130, v106, v106, v130\n\tv_dot2_f32_bf16 v129, v107, s76, v129\n\tv_dot2_f32_bf16 v131, v107, v107, v131\n\ts_waitcnt vmcnt(21)\n\tv_dot2_f32_bf16 v128, v108, s76, v128\n\tv_dot2_f32_bf16 v130, v108, v108, v130\n\tv_dot2_f32_bf16 v129, v109, s76, v129\n\tv_dot2_f32_bf16 v131, v109, v109, v131\n\tv_dot2_f32_bf16 v128, v110, s76, v128\n\tv_dot2_f32_bf16 v130, v110, v110, v130\n\tv_dot2_f32_bf16 v129, v111, s76, v129\n\tv_dot2_f32_bf16 v131, v111, v111, v131\n\ts_waitcnt vmcnt(20)\n\tv_dot2_f32_bf16 v128, v112, s76, v128\n\tv_dot2_f32_bf16 v130, v112, v112, v130\n\tv_dot2_f32_bf16 v129, v113, s76, v129\n\tv_dot2_f32_bf16 v131, v113, v113, v131\n\tv_dot2_f32_bf16 v128, v114, s76, v128\n\tv_dot2_f32_bf16 v130, v114, v114, v130\n\tv_dot2_f32_bf16 v129, v115, s76, v129\n\tv_dot2_f32_bf16 v131, v115, v115, v131\n\tv_add_f32 v124, v124, v125\n\tv_add_f32 v126, v126, v127\n\tv_add_f32 v128, v128, v129\n\tv_add_f32 v130, v130, v131\n\tv_add_f32_dpp v124, v124, v124 quad_perm:[1,0,3,2] row_mask:0xf bank_mask:0xf\n\tv_add_f32_dpp v126, v126, v126 quad_perm:[1,0,3,2] row_mask:0xf bank_mask:0xf\n\tv_add_f32_dpp v128, v128, v128 quad_perm:[1,0,3,2] row_mask:0xf bank_mask:0xf\n\tv_add_f32_dpp v130, v130, v130 quad_perm:[1,0,3,2] row_mask:0xf bank_mask:0xf\n\tv_add_f32_dpp v124, v124, v124 quad_perm:[2,3,0,1] row_mask:0xf bank_mask:0xf\n\tv_add_f32_dpp v126, v126, v126 quad_perm:[2,3,0,1] row_mask:0xf bank_mask:0xf\n\tv_add_f32_dpp v128, v128, v128 quad_perm:[2,3,0,1] row_mask:0xf bank_mask:0xf\n\tv_add_f32_dpp v130, v130, v130 quad_perm:[2,3,0,1] row_mask:0xf bank_mask:0xf\n\tv_add_f32_dpp v124, v124, v124 row_half_mirror row_mask:0xf bank_mask:0xf\n\tv_add_f32_dpp v126, v126, v126 row_half_mirror row_mask:0xf bank_mask:0xf\n\tv_add_f32_dpp v128, v128, v128 row_half_mirror row_mask:0xf bank_mask:0xf\n\tv_add_f32_dpp v130, v130, v130 row_half_mirror row_mask:0xf bank_mask:0xf\n\ts_mov_b32 s70, 0x3ae38e39\n\ts_mov_b32 s71, 0x3727c5ac\n\ts_mov_b32 s96, 0x3fb8aa3b\n\tv_mul_f32 v124, s70, v124\n\tv_mul_f32 v126, s70, v126\n\tv_fma_f32 v126, -v124, v124, v126\n\tv_max_f32 v126, 0, v126\n\tv_cvt_pk_bf16_f32 v124, v124, v124\n\tv_lshlrev_b32 v124, 16, v124\n\tv_cvt_pk_bf16_f32 v126, v126, v126\n\tv_lshlrev_b32 v126, 16, v126\n\tv_add_f32 v126, s71, v126\n\tv_cvt_pk_bf16_f32 v126, v126, v126\n\tv_lshlrev_b32 v126, 16, v126\n\tv_sqrt_f32 v126, v126\n\ts_nop 0\n\tv_cvt_pk_bf16_f32 v126, v126, v126\n\tv_lshlrev_b32 v126, 16, v126\n\tv_rcp_f32 v132, v126\n\ts_nop 0\n\tv_mul_f32 v132, s96, v132\n\tv_mov_b32 v133, v132\n\tv_mul_f32 v134, -v124, v132\n\tv_mov_b32 v135, v134\n\tv_mul_f32 v128, s70, v128\n\tv_mul_f32 v130, s70, v130\n\tv_fma_f32 v130, -v128, v128, v130\n\tv_max_f32 v130, 0, v130\n\tv_cvt_pk_bf16_f32 v128, v128, v128\n\tv_lshlrev_b32 v128, 16, v128\n\tv_cvt_pk_bf16_f32 v130, v130, v130\n\tv_lshlrev_b32 v130, 16, v130\n\tv_add_f32 v130, s71, v130\n\tv_cvt_pk_bf16_f32 v130, v130, v130\n\tv_lshlrev_b32 v130, 16, v130\n\tv_sqrt_f32 v130, v130\n\ts_nop 0\n\tv_cvt_pk_bf16_f32 v130, v130, v130\n\tv_lshlrev_b32 v130, 16, v130\n\tv_rcp_f32 v136, v130\n\ts_nop 0\n\tv_mul_f32 v136, s96, v136\n\tv_mov_b32 v137, v136\n\tv_mul_f32 v138, -v128, v136\n\tv_mov_b32 v139, v138\n\tv_lshlrev_b32 v140, 16, v44\n\tv_and_b32 v141, s77, v44\n\tv_lshlrev_b32 v142, 16, v45\n\tv_and_b32 v143, s77, v45\n\tv_lshlrev_b32 v144, 16, v46\n\tv_and_b32 v145, s77, v46\n\tv_lshlrev_b32 v146, 16, v47\n\tv_and_b32 v147, s77, v47\n\tv_pk_fma_f32 v[140:141], v[140:141], v[132:133], v[134:135]\n\tv_pk_fma_f32 v[142:143], v[142:143], v[132:133], v[134:135]\n\tv_pk_fma_f32 v[144:145], v[144:145], v[132:133], v[134:135]\n\tv_pk_fma_f32 v[146:147], v[146:147], v[132:133], v[134:135]\n\tv_cvt_pk_bf16_f32 v44, v140, v141\n\tv_cvt_pk_bf16_f32 v45, v142, v143\n\tv_cvt_pk_bf16_f32 v46, v144, v145\n\tv_cvt_pk_bf16_f32 v47, v146, v147\n\tds_write_b128 v188, v[44:47] offset:0\n\tv_lshlrev_b32 v140, 16, v80\n\tv_and_b32 v141, s77, v80\n\tv_lshlrev_b32 v142, 16, v81\n\tv_and_b32 v143, s77, v81\n\tv_lshlrev_b32 v144, 16, v82\n\tv_and_b32 v145, s77, v82\n\tv_lshlrev_b32 v146, 16, v83\n\tv_and_b32 v147, s77, v83\n\tv_pk_fma_f32 v[140:141], v[140:141], v[136:137], v[138:139]\n\tv_pk_fma_f32 v[142:143], v[142:143], v[136:137], v[138:139]\n\tv_pk_fma_f32 v[144:145], v[144:145], v[136:137], v[138:139]\n\tv_pk_fma_f32 v[146:147], v[146:147], v[136:137], v[138:139]\n\tv_cvt_pk_bf16_f32 v80, v140, v141\n\tv_cvt_pk_bf16_f32 v81, v142, v143\n\tv_cvt_pk_bf16_f32 v82, v144, v145\n\tv_cvt_pk_bf16_f32 v83, v146, v147\n\tds_write_b128 v189, v[80:83] offset:0\n\tv_lshlrev_b32 v140, 16, v48\n\tv_and_b32 v141, s77, v48\n\tv_lshlrev_b32 v142, 16, v49\n\tv_and_b32 v143, s77, v49\n\tv_lshlrev_b32 v144, 16, v50\n\tv_and_b32 v145, s77, v50\n\tv_lshlrev_b32 v146, 16, v51\n\tv_and_b32 v147, s77, v51\n\tv_pk_fma_f32 v[140:141], v[140:141], v[132:133], v[134:135]\n\tv_pk_fma_f32 v[142:143], v[142:143], v[132:133], v[134:135]\n\tv_pk_fma_f32 v[144:145], v[144:145], v[132:133], v[134:135]\n\tv_pk_fma_f32 v[146:147], v[146:147], v[132:133], v[134:135]\n\tv_cvt_pk_bf16_f32 v48, v140, v141\n\tv_cvt_pk_bf16_f32 v49, v142, v143\n\tv_cvt_pk_bf16_f32 v50, v144, v145\n\tv_cvt_pk_bf16_f32 v51, v146, v147\n\tds_write_b128 v188, v[48:51] offset:1024\n\tv_lshlrev_b32 v140, 16, v84\n\tv_and_b32 v141, s77, v84\n\tv_lshlrev_b32 v142, 16, v85\n\tv_and_b32 v143, s77, v85\n\tv_lshlrev_b32 v144, 16, v86\n\tv_and_b32 v145, s77, v86\n\tv_lshlrev_b32 v146, 16, v87\n\tv_and_b32 v147, s77, v87\n\tv_pk_fma_f32 v[140:141], v[140:141], v[136:137], v[138:139]\n\tv_pk_fma_f32 v[142:143], v[142:143], v[136:137], v[138:139]\n\tv_pk_fma_f32 v[144:145], v[144:145], v[136:137], v[138:139]\n\tv_pk_fma_f32 v[146:147], v[146:147], v[136:137], v[138:139]\n\tv_cvt_pk_bf16_f32 v84, v140, v141\n\tv_cvt_pk_bf16_f32 v85, v142, v143\n\tv_cvt_pk_bf16_f32 v86, v144, v145\n\tv_cvt_pk_bf16_f32 v87, v146, v147\n\tds_write_b128 v189, v[84:87] offset:1024\n\tv_lshlrev_b32 v140, 16, v52\n\tv_and_b32 v141, s77, v52\n\tv_lshlrev_b32 v142, 16, v53\n\tv_and_b32 v143, s77, v53\n\tv_lshlrev_b32 v144, 16, v54\n\tv_and_b32 v145, s77, v54\n\tv_lshlrev_b32 v146, 16, v55\n\tv_and_b32 v147, s77, v55\n\tv_pk_fma_f32 v[140:141], v[140:141], v[132:133], v[134:135]\n\tv_pk_fma_f32 v[142:143], v[142:143], v[132:133], v[134:135]\n\tv_pk_fma_f32 v[144:145], v[144:145], v[132:133], v[134:135]\n\tv_pk_fma_f32 v[146:147], v[146:147], v[132:133], v[134:135]\n\tv_cvt_pk_bf16_f32 v52, v140, v141\n\tv_cvt_pk_bf16_f32 v53, v142, v143\n\tv_cvt_pk_bf16_f32 v54, v144, v145\n\tv_cvt_pk_bf16_f32 v55, v146, v147\n\tds_write_b128 v188, v[52:55] offset:2048\n\tv_lshlrev_b32 v140, 16, v88\n\tv_and_b32 v141, s77, v88\n\tv_lshlrev_b32 v142, 16, v89\n\tv_and_b32 v143, s77, v89\n\tv_lshlrev_b32 v144, 16, v90\n\tv_and_b32 v145, s77, v90\n\tv_lshlrev_b32 v146, 16, v91\n\tv_and_b32 v147, s77, v91\n\tv_pk_fma_f32 v[140:141], v[140:141], v[136:137], v[138:139]\n\tv_pk_fma_f32 v[142:143], v[142:143], v[136:137], v[138:139]\n\tv_pk_fma_f32 v[144:145], v[144:145], v[136:137], v[138:139]\n\tv_pk_fma_f32 v[146:147], v[146:147], v[136:137], v[138:139]\n\tv_cvt_pk_bf16_f32 v88, v140, v141\n\tv_cvt_pk_bf16_f32 v89, v142, v143\n\tv_cvt_pk_bf16_f32 v90, v144, v145\n\tv_cvt_pk_bf16_f32 v91, v146, v147\n\tds_write_b128 v189, v[88:91] offset:2048\n\tv_lshlrev_b32 v140, 16, v56\n\tv_and_b32 v141, s77, v56\n\tv_lshlrev_b32 v142, 16, v57\n\tv_and_b32 v143, s77, v57\n\tv_lshlrev_b32 v144, 16, v58\n\tv_and_b32 v145, s77, v58\n\tv_lshlrev_b32 v146, 16, v59\n\tv_and_b32 v147, s77, v59\n\tv_pk_fma_f32 v[140:141], v[140:141], v[132:133], v[134:135]\n\tv_pk_fma_f32 v[142:143], v[142:143], v[132:133], v[134:135]\n\tv_pk_fma_f32 v[144:145], v[144:145], v[132:133], v[134:135]\n\tv_pk_fma_f32 v[146:147], v[146:147], v[132:133], v[134:135]\n\tv_cvt_pk_bf16_f32 v56, v140, v141\n\tv_cvt_pk_bf16_f32 v57, v142, v143\n\tv_cvt_pk_bf16_f32 v58, v144, v145\n\tv_cvt_pk_bf16_f32 v59, v146, v147\n\tds_write_b128 v188, v[56:59] offset:3072\n\tv_lshlrev_b32 v140, 16, v92\n\tv_and_b32 v141, s77, v92\n\tv_lshlrev_b32 v142, 16, v93\n\tv_and_b32 v143, s77, v93\n\tv_lshlrev_b32 v144, 16, v94\n\tv_and_b32 v145, s77, v94\n\tv_lshlrev_b32 v146, 16, v95\n\tv_and_b32 v147, s77, v95\n\tv_pk_fma_f32 v[140:141], v[140:141], v[136:137], v[138:139]\n\tv_pk_fma_f32 v[142:143], v[142:143], v[136:137], v[138:139]\n\tv_pk_fma_f32 v[144:145], v[144:145], v[136:137], v[138:139]\n\tv_pk_fma_f32 v[146:147], v[146:147], v[136:137], v[138:139]\n\tv_cvt_pk_bf16_f32 v92, v140, v141\n\tv_cvt_pk_bf16_f32 v93, v142, v143\n\tv_cvt_pk_bf16_f32 v94, v144, v145\n\tv_cvt_pk_bf16_f32 v95, v146, v147\n\tds_write_b128 v189, v[92:95] offset:3072\n\tv_lshlrev_b32 v140, 16, v60\n\tv_and_b32 v141, s77, v60\n\tv_lshlrev_b32 v142, 16, v61\n\tv_and_b32 v143, s77, v61\n\tv_lshlrev_b32 v144, 16, v62\n\tv_and_b32 v145, s77, v62\n\tv_lshlrev_b32 v146, 16, v63\n\tv_and_b32 v147, s77, v63\n\tv_pk_fma_f32 v[140:141], v[140:141], v[132:133], v[134:135]\n\tv_pk_fma_f32 v[142:143], v[142:143], v[132:133], v[134:135]\n\tv_pk_fma_f32 v[144:145], v[144:145], v[132:133], v[134:135]\n\tv_pk_fma_f32 v[146:147], v[146:147], v[132:133], v[134:135]\n\tv_cvt_pk_bf16_f32 v60, v140, v141\n\tv_cvt_pk_bf16_f32 v61, v142, v143\n\tv_cvt_pk_bf16_f32 v62, v144, v145\n\tv_cvt_pk_bf16_f32 v63, v146, v147\n\tds_write_b128 v188, v[60:63] offset:4096\n\tv_lshlrev_b32 v140, 16, v96\n\tv_and_b32 v141, s77, v96\n\tv_lshlrev_b32 v142, 16, v97\n\tv_and_b32 v143, s77, v97\n\tv_lshlrev_b32 v144, 16, v98\n\tv_and_b32 v145, s77, v98\n\tv_lshlrev_b32 v146, 16, v99\n\tv_and_b32 v147, s77, v99\n\tv_pk_fma_f32 v[140:141], v[140:141], v[136:137], v[138:139]\n\tv_pk_fma_f32 v[142:143], v[142:143], v[136:137], v[138:139]\n\tv_pk_fma_f32 v[144:145], v[144:145], v[136:137], v[138:139]\n\tv_pk_fma_f32 v[146:147], v[146:147], v[136:137], v[138:139]\n\tv_cvt_pk_bf16_f32 v96, v140, v141\n\tv_cvt_pk_bf16_f32 v97, v142, v143\n\tv_cvt_pk_bf16_f32 v98, v144, v145\n\tv_cvt_pk_bf16_f32 v99, v146, v147\n\tds_write_b128 v189, v[96:99] offset:4096\n\tv_lshlrev_b32 v140, 16, v64\n\tv_and_b32 v141, s77, v64\n\tv_lshlrev_b32 v142, 16, v65\n\tv_and_b32 v143, s77, v65\n\tv_lshlrev_b32 v144, 16, v66\n\tv_and_b32 v145, s77, v66\n\tv_lshlrev_b32 v146, 16, v67\n\tv_and_b32 v147, s77, v67\n\tv_pk_fma_f32 v[140:141], v[140:141], v[132:133], v[134:135]\n\tv_pk_fma_f32 v[142:143], v[142:143], v[132:133], v[134:135]\n\tv_pk_fma_f32 v[144:145], v[144:145], v[132:133], v[134:135]\n\tv_pk_fma_f32 v[146:147], v[146:147], v[132:133], v[134:135]\n\tv_cvt_pk_bf16_f32 v64, v140, v141\n\tv_cvt_pk_bf16_f32 v65, v142, v143\n\tv_cvt_pk_bf16_f32 v66, v144, v145\n\tv_cvt_pk_bf16_f32 v67, v146, v147\n\tds_write_b128 v188, v[64:67] offset:5120\n\tv_lshlrev_b32 v140, 16, v100\n\tv_and_b32 v141, s77, v100\n\tv_lshlrev_b32 v142, 16, v101\n\tv_and_b32 v143, s77, v101\n\tv_lshlrev_b32 v144, 16, v102\n\tv_and_b32 v145, s77, v102\n\tv_lshlrev_b32 v146, 16, v103\n\tv_and_b32 v147, s77, v103\n\tv_pk_fma_f32 v[140:141], v[140:141], v[136:137], v[138:139]\n\tv_pk_fma_f32 v[142:143], v[142:143], v[136:137], v[138:139]\n\tv_pk_fma_f32 v[144:145], v[144:145], v[136:137], v[138:139]\n\tv_pk_fma_f32 v[146:147], v[146:147], v[136:137], v[138:139]\n\tv_cvt_pk_bf16_f32 v100, v140, v141\n\tv_cvt_pk_bf16_f32 v101, v142, v143\n\tv_cvt_pk_bf16_f32 v102, v144, v145\n\tv_cvt_pk_bf16_f32 v103, v146, v147\n\tds_write_b128 v189, v[100:103] offset:5120\n\tv_lshlrev_b32 v140, 16, v68\n\tv_and_b32 v141, s77, v68\n\tv_lshlrev_b32 v142, 16, v69\n\tv_and_b32 v143, s77, v69\n\tv_lshlrev_b32 v144, 16, v70\n\tv_and_b32 v145, s77, v70\n\tv_lshlrev_b32 v146, 16, v71\n\tv_and_b32 v147, s77, v71\n\tv_pk_fma_f32 v[140:141], v[140:141], v[132:133], v[134:135]\n\tv_pk_fma_f32 v[142:143], v[142:143], v[132:133], v[134:135]\n\tv_pk_fma_f32 v[144:145], v[144:145], v[132:133], v[134:135]\n\tv_pk_fma_f32 v[146:147], v[146:147], v[132:133], v[134:135]\n\tv_cvt_pk_bf16_f32 v68, v140, v141\n\tv_cvt_pk_bf16_f32 v69, v142, v143\n\tv_cvt_pk_bf16_f32 v70, v144, v145\n\tv_cvt_pk_bf16_f32 v71, v146, v147\n\tds_write_b128 v188, v[68:71] offset:6144\n\tv_lshlrev_b32 v140, 16, v104\n\tv_and_b32 v141, s77, v104\n\tv_lshlrev_b32 v142, 16, v105\n\tv_and_b32 v143, s77, v105\n\tv_lshlrev_b32 v144, 16, v106\n\tv_and_b32 v145, s77, v106\n\tv_lshlrev_b32 v146, 16, v107\n\tv_and_b32 v147, s77, v107\n\tv_pk_fma_f32 v[140:141], v[140:141], v[136:137], v[138:139]\n\tv_pk_fma_f32 v[142:143], v[142:143], v[136:137], v[138:139]\n\tv_pk_fma_f32 v[144:145], v[144:145], v[136:137], v[138:139]\n\tv_pk_fma_f32 v[146:147], v[146:147], v[136:137], v[138:139]\n\tv_cvt_pk_bf16_f32 v104, v140, v141\n\tv_cvt_pk_bf16_f32 v105, v142, v143\n\tv_cvt_pk_bf16_f32 v106, v144, v145\n\tv_cvt_pk_bf16_f32 v107, v146, v147\n\tds_write_b128 v189, v[104:107] offset:6144\n\tv_lshlrev_b32 v140, 16, v72\n\tv_and_b32 v141, s77, v72\n\tv_lshlrev_b32 v142, 16, v73\n\tv_and_b32 v143, s77, v73\n\tv_lshlrev_b32 v144, 16, v74\n\tv_and_b32 v145, s77, v74\n\tv_lshlrev_b32 v146, 16, v75\n\tv_and_b32 v147, s77, v75\n\tv_pk_fma_f32 v[140:141], v[140:141], v[132:133], v[134:135]\n\tv_pk_fma_f32 v[142:143], v[142:143], v[132:133], v[134:135]\n\tv_pk_fma_f32 v[144:145], v[144:145], v[132:133], v[134:135]\n\tv_pk_fma_f32 v[146:147], v[146:147], v[132:133], v[134:135]\n\tv_cvt_pk_bf16_f32 v72, v140, v141\n\tv_cvt_pk_bf16_f32 v73, v142, v143\n\tv_cvt_pk_bf16_f32 v74, v144, v145\n\tv_cvt_pk_bf16_f32 v75, v146, v147\n\tds_write_b128 v188, v[72:75] offset:7168\n\tv_lshlrev_b32 v140, 16, v108\n\tv_and_b32 v141, s77, v108\n\tv_lshlrev_b32 v142, 16, v109\n\tv_and_b32 v143, s77, v109\n\tv_lshlrev_b32 v144, 16, v110\n\tv_and_b32 v145, s77, v110\n\tv_lshlrev_b32 v146, 16, v111\n\tv_and_b32 v147, s77, v111\n\tv_pk_fma_f32 v[140:141], v[140:141], v[136:137], v[138:139]\n\tv_pk_fma_f32 v[142:143], v[142:143], v[136:137], v[138:139]\n\tv_pk_fma_f32 v[144:145], v[144:145], v[136:137], v[138:139]\n\tv_pk_fma_f32 v[146:147], v[146:147], v[136:137], v[138:139]\n\tv_cvt_pk_bf16_f32 v108, v140, v141\n\tv_cvt_pk_bf16_f32 v109, v142, v143\n\tv_cvt_pk_bf16_f32 v110, v144, v145\n\tv_cvt_pk_bf16_f32 v111, v146, v147\n\tds_write_b128 v189, v[108:111] offset:7168\n\tv_lshlrev_b32 v140, 16, v76\n\tv_and_b32 v141, s77, v76\n\tv_lshlrev_b32 v142, 16, v77\n\tv_and_b32 v143, s77, v77\n\tv_lshlrev_b32 v144, 16, v78\n\tv_and_b32 v145, s77, v78\n\tv_lshlrev_b32 v146, 16, v79\n\tv_and_b32 v147, s77, v79\n\tv_pk_fma_f32 v[140:141], v[140:141], v[132:133], v[134:135]\n\tv_pk_fma_f32 v[142:143], v[142:143], v[132:133], v[134:135]\n\tv_pk_fma_f32 v[144:145], v[144:145], v[132:133], v[134:135]\n\tv_pk_fma_f32 v[146:147], v[146:147], v[132:133], v[134:135]\n\tv_cvt_pk_bf16_f32 v76, v140, v141\n\tv_cvt_pk_bf16_f32 v77, v142, v143\n\tv_cvt_pk_bf16_f32 v78, v144, v145\n\tv_cvt_pk_bf16_f32 v79, v146, v147\n\tds_write_b128 v188, v[76:79] offset:8192\n\tv_lshlrev_b32 v140, 16, v112\n\tv_and_b32 v141, s77, v112\n\tv_lshlrev_b32 v142, 16, v113\n\tv_and_b32 v143, s77, v113\n\tv_lshlrev_b32 v144, 16, v114\n\tv_and_b32 v145, s77, v114\n\tv_lshlrev_b32 v146, 16, v115\n\tv_and_b32 v147, s77, v115\n\tv_pk_fma_f32 v[140:141], v[140:141], v[136:137], v[138:139]\n\tv_pk_fma_f32 v[142:143], v[142:143], v[136:137], v[138:139]\n\tv_pk_fma_f32 v[144:145], v[144:145], v[136:137], v[138:139]\n\tv_pk_fma_f32 v[146:147], v[146:147], v[136:137], v[138:139]\n\tv_cvt_pk_bf16_f32 v112, v140, v141\n\tv_cvt_pk_bf16_f32 v113, v142, v143\n\tv_cvt_pk_bf16_f32 v114, v144, v145\n\tv_cvt_pk_bf16_f32 v115, v146, v147\n\tds_write_b128 v189, v[112:115] offset:8192\n\ts_waitcnt vmcnt(7)\n\tv_lshlrev_b32 v140, 16, v186\n\tv_and_b32 v141, s77, v186\n\tv_lshlrev_b32 v142, 16, v187\n\tv_and_b32 v143, s77, v187\n\tv_mul_f32 v140, s96, v140\n\tv_mul_f32 v141, s96, v141\n\tv_mul_f32 v142, s96, v142\n\tv_mul_f32 v143, s96, v143\n\ts_mov_b32 s94, 147456\n\tv_lshl_add_u32 v172, v185, 4, s94\n\tv_min_u32 v172, 150000, v172\n\tds_write_b128 v172, v[140:143]\n\ts_waitcnt lgkmcnt(0)\n\ts_waitcnt vmcnt(5)\n\tv_readfirstlane_b32 s88, v220\n\tv_readfirstlane_b32 s89, v221\n\ts_nop 2\n\ts_add_u32 s88, s88, 1\n\ts_addc_u32 s89, s89, 0\n\ts_barrier\n\tds_read_b128 a[0:3], v25 offset:0\n\tds_read_b128 a[8:11], v26 offset:0\n\tds_read_b128 a[16:19], v27 offset:0\n\tds_read_b128 a[24:27], v28 offset:0\n\tds_read_b128 a[32:35], v25 offset:1024\n\tds_read_b128 a[40:43], v26 offset:1024\n\tds_read_b128 a[48:51], v27 offset:1024\n\tds_read_b128 a[56:59], v28 offset:1024\n\tds_read_b128 a[64:67], v25 offset:2048\n\tds_read_b128 a[72:75], v26 offset:2048\n\tds_read_b128 a[80:83], v27 offset:2048\n\tds_read_b128 a[88:91], v28 offset:2048\n\tds_read_b64_tr_b16 v[44:45], v12 offset:0\n\tds_read_b64_tr_b16 v[46:47], v12 offset:512\n\tds_read_b64_tr_b16 v[48:49], v12 offset:36864\n\tds_read_b64_tr_b16 v[50:51], v12 offset:37376\n\tds_read_b64_tr_b16 v[52:53], v12 offset:1024\n\tds_read_b64_tr_b16 v[54:55], v12 offset:1536\n\tds_read_b64_tr_b16 v[56:57], v12 offset:37888\n\tds_read_b64_tr_b16 v[58:59], v12 offset:38400\n\tds_read_b64_tr_b16 v[60:61], v12 offset:2048\n\tds_read_b64_tr_b16 v[62:63], v12 offset:2560\n\tds_read_b64_tr_b16 v[64:65], v12 offset:38912\n\tds_read_b64_tr_b16 v[66:67], v12 offset:39424\n\tds_read_b64_tr_b16 v[68:69], v12 offset:3072\n\tds_read_b64_tr_b16 v[70:71], v12 offset:3584\n\tds_read_b64_tr_b16 v[72:73], v12 offset:39936\n\tds_read_b64_tr_b16 v[74:75], v12 offset:40448\n\tds_read_b64_tr_b16 v[76:77], v12 offset:4096\n\tds_read_b64_tr_b16 v[78:79], v12 offset:4608\n\tds_read_b64_tr_b16 v[80:81], v12 offset:40960\n\tds_read_b64_tr_b16 v[82:83], v12 offset:41472\n\tds_read_b64_tr_b16 v[84:85], v12 offset:5120\n\tds_read_b64_tr_b16 v[86:87], v12 offset:5632\n\tds_read_b64_tr_b16 v[88:89], v12 offset:41984\n\tds_read_b64_tr_b16 v[90:91], v12 offset:42496\n\ts_waitcnt lgkmcnt(15)\n\tv_mfma_f32_32x32x16_bf16 v[92:107], v[44:47], a[0:3], 0\n\ts_add_u32 m0, s64, 12288\n\tds_read_b128 a[96:99], v25 offset:3072\n\ts_add_u32 s98, s98, 0x3000\n\ts_addc_u32 s99, s99, 0\n\tds_read_b128 a[104:107], v26 offset:3072\n\tglobal_load_lds_dwordx4 v8, s[98:99] offset:0\n\tv_mfma_f32_32x32x16_bf16 v[92:107], v[48:51], a[8:11], v[92:107]\n\tds_read_b128 a[112:115], v27 offset:3072\n\tglobal_load_lds_dwordx4 v9, s[98:99] offset:1024\n\tds_read_b128 a[120:123], v28 offset:3072\n\tv_mfma_f32_32x32x16_bf16 v[92:107], v[52:55], a[16:19], v[92:107]\n\tds_read_b128 a[128:131], v25 offset:4096\n\tglobal_load_lds_dwordx4 v10, s[98:99] offset:2048\n\tds_read_b128 a[136:139], v26 offset:4096\n\tv_mfma_f32_32x32x16_bf16 v[92:107], v[56:59], a[24:27], v[92:107]\n\ts_add_u32 m0, s64, 15360\n\tds_read_b128 a[144:147], v27 offset:4096\n\ts_add_u32 s98, s98, 0x3000\n\ts_addc_u32 s99, s99, 0\n\tds_read_b128 a[152:155], v28 offset:4096\n\tglobal_load_lds_dwordx4 v8, s[98:99] offset:0\n\ts_waitcnt lgkmcnt(15)\n\tv_mfma_f32_32x32x16_bf16 v[92:107], v[60:63], a[32:35], v[92:107]\n\tds_read_b128 a[160:163], v25 offset:5120\n\tglobal_load_lds_dwordx4 v9, s[98:99] offset:1024\n\tds_read_b128 a[168:171], v26 offset:5120\n\tv_mfma_f32_32x32x16_bf16 v[92:107], v[64:67], a[40:43], v[92:107]\n\tds_read_b128 a[176:179], v27 offset:5120\n\tglobal_load_lds_dwordx4 v10, s[98:99] offset:2048\n\tds_read_b128 a[184:187], v28 offset:5120\n\ts_waitcnt lgkmcnt(12)\n\ts_barrier\n\tv_mfma_f32_32x32x16_bf16 v[92:107], v[68:71], a[48:51], v[92:107]\n\tds_read_b64_tr_b16 v[44:45], v12 offset:6144\n\tds_read_b64_tr_b16 v[48:49], v12 offset:43008\n\tds_read_b128 a[192:195], v25 offset:6144\n\tv_lshlrev_b32 v8, 4, v0\n\tds_read_b128 a[200:203], v26 offset:6144\n\tv_lshl_add_u32 v8, s62, 10, v8\n\tv_mfma_f32_32x32x16_bf16 v[92:107], v[72:75], a[56:59], v[92:107]\n\tds_read_b64_tr_b16 v[46:47], v12 offset:6656\n\tds_read_b64_tr_b16 v[50:51], v12 offset:43520\n\tds_read_b64_tr_b16 v[52:53], v12 offset:7168\n\tds_read_b128 a[208:211], v27 offset:6144\n\tv_add_u32 v9, 3072, v8\n\tv_add_u32 v10, 6144, v8\n\tv_mfma_f32_32x32x16_bf16 v[92:107], v[76:79], a[64:67], v[92:107]\n\tds_read_b64_tr_b16 v[54:55], v12 offset:7680\n\tds_read_b64_tr_b16 v[56:57], v12 offset:44032\n\tds_read_b128 a[216:219], v28 offset:6144\n\ts_add_u32 m0, s64, 0\n\tds_read_b128 a[224:227], v25 offset:7168\n\tds_read_b128 a[232:235], v26 offset:7168\n\tv_mfma_f32_32x32x16_bf16 v[92:107], v[80:83], a[72:75], v[92:107]\n\tds_read_b64_tr_b16 v[58:59], v12 offset:44544\n\tds_read_b64_tr_b16 v[60:61], v12 offset:8192\n\ts_add_u32 s98, s85, 0x0\n\ts_add_u32 s98, s98, s24\n\ts_addc_u32 s99, s25, 0\n\tds_read_b128 a[240:243], v27 offset:7168\n\tv_mfma_f32_32x32x16_bf16 v[92:107], v[84:87], a[80:83], v[92:107]\n\tds_read_b64_tr_b16 v[62:63], v12 offset:8704\n\tds_read_b64_tr_b16 v[64:65], v12 offset:45056\n\tglobal_load_lds_dwordx4 v8, s[98:99] offset:0\n\tv_mfma_f32_32x32x16_bf16 v[92:107], v[88:91], a[88:91], v[92:107]\n\tds_read_b64_tr_b16 v[66:67], v12 offset:45568\n\tds_read_b64_tr_b16 v[68:69], v12 offset:9216\n\tds_read_b128 a[248:251], v28 offset:7168\n\tds_read_b128 v[224:227], v25 offset:8192\n\tds_read_b128 v[232:235], v26 offset:8192\n\tds_read_b128 v[240:243], v27 offset:8192\n\ts_waitcnt lgkmcnt(15)\n\tv_mfma_f32_32x32x16_bf16 v[92:107], v[44:47], a[96:99], v[92:107]\n\tds_read_b64_tr_b16 v[70:71], v12 offset:9728\n\tds_read_b128 v[248:251], v28 offset:8192\n\tds_read_b64_tr_b16 v[72:73], v12 offset:46080\n\tds_read_b64_tr_b16 v[76:77], v12 offset:10240\n\tglobal_load_lds_dwordx4 v9, s[98:99] offset:1024\n\tv_mfma_f32_32x32x16_bf16 v[92:107], v[48:51], a[104:107], v[92:107]\n\tds_read_b64_tr_b16 v[74:75], v12 offset:46592\n\tds_read_b64_tr_b16 v[78:79], v12 offset:10752\n\tds_read_b64_tr_b16 v[80:81], v12 offset:47104\n\tv_mfma_f32_32x32x16_bf16 v[92:107], v[52:55], a[112:115], v[92:107]\n\tds_read_b64_tr_b16 v[82:83], v12 offset:47616\n\tds_read_b64_tr_b16 v[84:85], v12 offset:11264\n\tglobal_load_lds_dwordx4 v10, s[98:99] offset:2048\n\ts_waitcnt lgkmcnt(15)\n\tv_mfma_f32_32x32x16_bf16 v[92:107], v[56:59], a[120:123], v[92:107]\n\tds_read_b64_tr_b16 v[86:87], v12 offset:11776\n\tds_read_b64_tr_b16 v[88:89], v12 offset:48128\n\ts_add_u32 m0, s64, 3072\n\ts_add_u32 s98, s98, 0x3000\n\ts_addc_u32 s99, s99, 0\n\tglobal_load_lds_dwordx4 v8, s[98:99] offset:0\n\tv_mfma_f32_32x32x16_bf16 v[92:107], v[60:63], a[128:131], v[92:107]\n\tds_read_b64_tr_b16 v[90:91], v12 offset:48640\n\tglobal_load_lds_dwordx4 v9, s[98:99] offset:1024\n\ts_waitcnt lgkmcnt(15)\n\tv_mfma_f32_32x32x16_bf16 v[92:107], v[64:67], a[136:139], v[92:107]\n\tglobal_load_lds_dwordx4 v10, s[98:99] offset:2048\n\ts_waitcnt vmcnt(6) lgkmcnt(0)\n\ts_barrier\n\tv_mfma_f32_32x32x16_bf16 v[92:107], v[68:71], a[144:147], v[92:107]\n\tds_read_b64_tr_b16 v[44:45], v12 offset:12288\n\tds_read_b64_tr_b16 v[48:49], v12 offset:49152\n\ts_add_u32 m0, s64, 6144\n\tds_read_b128 a[4:7], v29 offset:0\n\ts_add_u32 s98, s98, 0x3000\n\ts_addc_u32 s99, s99, 0\n\tv_mfma_f32_32x32x16_bf16 v[92:107], v[72:75], a[152:155], v[92:107]\n\tds_read_b64_tr_b16 v[46:47], v12 offset:12800\n\tds_read_b64_tr_b16 v[50:51], v12 offset:49664\n\tds_read_b64_tr_b16 v[52:53], v12 offset:13312\n\tds_read_b128 a[12:15], v30 offset:0\n\tv_mfma_f32_32x32x16_bf16 v[92:107], v[76:79], a[160:163], v[92:107]\n\tds_read_b64_tr_b16 v[54:55], v12 offset:13824\n\tds_read_b64_tr_b16 v[56:57], v12 offset:50176\n\tds_read_b128 a[20:23], v31 offset:0\n\tv_mfma_f32_32x32x16_bf16 v[92:107], v[80:83], a[168:171], v[92:107]\n\tds_read_b64_tr_b16 v[58:59], v12 offset:50688\n\tds_read_b64_tr_b16 v[60:61], v12 offset:14336\n\tglobal_load_lds_dwordx4 v8, s[98:99] offset:0\n\tv_mfma_f32_32x32x16_bf16 v[92:107], v[84:87], a[176:179], v[92:107]\n\tds_read_b64_tr_b16 v[62:63], v12 offset:14848\n\tds_read_b64_tr_b16 v[64:65], v12 offset:51200\n\tds_read_b128 a[28:31], v32 offset:0\n\tds_read_b128 a[36:39], v29 offset:1024\n\tv_mfma_f32_32x32x16_bf16 v[92:107], v[88:91], a[184:187], v[92:107]\n\tds_read_b64_tr_b16 v[66:67], v12 offset:51712\n\tds_read_b64_tr_b16 v[68:69], v12 offset:15360\n\tds_read_b128 a[44:47], v30 offset:1024\n\ts_waitcnt lgkmcnt(15)\n\tv_mfma_f32_32x32x16_bf16 v[92:107], v[44:47], a[192:195], v[92:107]\n\tds_read_b64_tr_b16 v[70:71], v12 offset:15872\n\tds_read_b64_tr_b16 v[72:73], v12 offset:52224\n\tds_read_b64_tr_b16 v[76:77], v12 offset:16384\n\tds_read_b128 a[52:55], v31 offset:1024\n\ts_waitcnt lgkmcnt(15)\n\tv_mfma_f32_32x32x16_bf16 v[92:107], v[48:51], a[200:203], v[92:107]\n\tds_read_b64_tr_b16 v[74:75], v12 offset:52736\n\tds_read_b64_tr_b16 v[78:79], v12 offset:16896\n\tds_read_b64_tr_b16 v[80:81], v12 offset:53248\n\tds_read_b128 a[60:63], v32 offset:1024\n\tv_mfma_f32_32x32x16_bf16 v[92:107], v[52:55], a[208:211], v[92:107]\n\tds_read_b64_tr_b16 v[82:83], v12 offset:53760\n\tds_read_b64_tr_b16 v[84:85], v12 offset:17408\n\tglobal_load_lds_dwordx4 v9, s[98:99] offset:1024\n\ts_waitcnt lgkmcnt(15)\n\tv_mfma_f32_32x32x16_bf16 v[92:107], v[56:59], a[216:219], v[92:107]\n\tds_read_b64_tr_b16 v[86:87], v12 offset:17920\n\tds_read_b64_tr_b16 v[88:89], v12 offset:54272\n\tds_read_b128 a[68:71], v29 offset:2048\n\tds_read_b128 a[76:79], v30 offset:2048\n\tv_mfma_f32_32x32x16_bf16 v[92:107], v[60:63], a[224:227], v[92:107]\n\tds_read_b64_tr_b16 v[90:91], v12 offset:54784\n\tglobal_load_lds_dwordx4 v10, s[98:99] offset:2048\n\tds_read_b128 a[84:87], v31 offset:2048\n\ts_waitcnt lgkmcnt(15)\n\tv_mfma_f32_32x32x16_bf16 v[92:107], v[64:67], a[232:235], v[92:107]\n\tds_read_b128 a[92:95], v32 offset:2048\n\ts_waitcnt vmcnt(3) lgkmcnt(2)\n\ts_barrier\n\tv_mfma_f32_32x32x16_bf16 v[92:107], v[68:71], a[240:243], v[92:107]\n\tds_read_b64_tr_b16 v[44:45], v17 offset:0\n\tds_read_b64_tr_b16 v[48:49], v17 offset:18432\n\tv_mul_u32_u24 v5, 9216, v1\n\tds_read_b128 a[100:103], v29 offset:3072\n\tv_add_u32 v5, v5, v2\n\tv_mfma_f32_32x32x16_bf16 v[92:107], v[72:75], a[248:251], v[92:107]\n\tds_read_b64_tr_b16 v[46:47], v17 offset:256\n\tds_read_b64_tr_b16 v[50:51], v17 offset:18688\n\tds_read_b64_tr_b16 v[52:53], v18 offset:0\n\tv_add_u32 v6, 146432, v5\n\tv_add_u32 v7, 292864, v5\n\tv_mfma_f32_32x32x16_bf16 v[92:107], v[76:79], v[224:227], v[92:107]\n\tds_read_b64_tr_b16 v[54:55], v18 offset:256\n\tds_read_b64_tr_b16 v[56:57], v18 offset:18432\n\tds_read_b128 a[108:111], v30 offset:3072\n\ts_add_u32 m0, s64, 12288\n\tv_mfma_f32_32x32x16_bf16 v[92:107], v[80:83], v[232:235], v[92:107]\n\tds_read_b64_tr_b16 v[58:59], v18 offset:18688\n\tds_read_b64_tr_b16 v[60:61], v17 offset:1024\n\ts_add_u32 s98, s80, 0x0\n\ts_add_u32 s98, s98, s44\n\ts_addc_u32 s99, s45, 0\n\tv_mfma_f32_32x32x16_bf16 v[92:107], v[84:87], v[240:243], v[92:107]\n\tds_read_b64_tr_b16 v[62:63], v17 offset:1280\n\tds_read_b64_tr_b16 v[64:65], v17 offset:19456\n\tds_read_b128 a[116:119], v31 offset:3072\n\tv_mfma_f32_32x32x16_bf16 v[92:107], v[88:91], v[248:251], v[92:107]\n\tds_read_b64_tr_b16 v[66:67], v17 offset:19712\n\tds_read_b64_tr_b16 v[68:69], v18 offset:1024\n\tglobal_load_lds_dwordx4 v5, s[98:99] offset:0\n\ts_waitcnt lgkmcnt(12)\n\tv_mfma_f32_16x16x32_bf16 v[108:111], v[44:47], a[0:3], 0\n\tds_read_b64_tr_b16 v[70:71], v18 offset:1280\n\tds_read_b64_tr_b16 v[72:73], v18 offset:19456\n\tds_read_b128 a[124:127], v32 offset:3072\n\ts_waitcnt lgkmcnt(14)\n\tv_mfma_f32_16x16x32_bf16 v[108:111], v[48:51], a[8:11], v[108:111]\n\tds_read_b64_tr_b16 v[74:75], v18 offset:19712\n\tds_read_b64_tr_b16 v[76:77], v17 offset:2048\n\tglobal_load_lds_dwordx4 v6, s[98:99] offset:1024\n\ts_waitcnt lgkmcnt(14)\n\tv_mfma_f32_16x16x32_bf16 v[108:111], v[52:55], a[16:19], v[108:111]\n\tds_read_b64_tr_b16 v[78:79], v17 offset:2304\n\tds_read_b64_tr_b16 v[80:81], v17 offset:20480\n\tds_read_b128 a[132:135], v29 offset:4096\n\ts_waitcnt lgkmcnt(14)\n\tv_mfma_f32_16x16x32_bf16 v[108:111], v[56:59], a[24:27], v[108:111]\n\tds_read_b64_tr_b16 v[82:83], v17 offset:20736\n\tds_read_b64_tr_b16 v[84:85], v18 offset:2048\n\ts_waitcnt lgkmcnt(14)\n\tv_mfma_f32_16x16x32_bf16 v[108:111], v[60:63], a[32:35], v[108:111]\n\tds_read_b64_tr_b16 v[86:87], v18 offset:2304\n\tds_read_b64_tr_b16 v[88:89], v18 offset:20480\n\tds_read_b128 a[140:143], v30 offset:4096\n\ts_waitcnt lgkmcnt(14)\n\tv_mfma_f32_16x16x32_bf16 v[108:111], v[64:67], a[40:43], v[108:111]\n\tds_read_b64_tr_b16 v[90:91], v18 offset:20736\n\tds_read_b64_tr_b16 v[44:45], v17 offset:3072\n\tglobal_load_lds_dwordx4 v7, s[98:99] offset:2048\n\ts_waitcnt lgkmcnt(14)\n\tv_mfma_f32_16x16x32_bf16 v[108:111], v[68:71], a[48:51], v[108:111]\n\tds_read_b64_tr_b16 v[46:47], v17 offset:3328\n\tds_read_b64_tr_b16 v[48:49], v17 offset:21504\n\tds_read_b64_tr_b16 v[52:53], v18 offset:3072\n\tds_read_b128 a[148:151], v31 offset:4096\n\ts_waitcnt lgkmcnt(15)\n\tv_mfma_f32_16x16x32_bf16 v[108:111], v[72:75], a[56:59], v[108:111]\n\tds_read_b64_tr_b16 v[50:51], v17 offset:21760\n\tds_read_b64_tr_b16 v[54:55], v18 offset:3328\n\tds_read_b64_tr_b16 v[56:57], v18 offset:21504\n\ts_add_u32 m0, s64, 15360\n\ts_add_u32 s98, s98, 0x6c000\n\ts_addc_u32 s99, s99, 0\n\ts_waitcnt lgkmcnt(15)\n\tv_mfma_f32_16x16x32_bf16 v[108:111], v[76:79], a[64:67], v[108:111]\n\tds_read_b64_tr_b16 v[58:59], v18 offset:21760\n\tds_read_b64_tr_b16 v[60:61], v17 offset:4096\n\tds_read_b128 a[156:159], v32 offset:4096\n\ts_waitcnt lgkmcnt(15)\n\tv_mfma_f32_16x16x32_bf16 v[108:111], v[80:83], a[72:75], v[108:111]\n\tds_read_b64_tr_b16 v[62:63], v17 offset:4352\n\tds_read_b64_tr_b16 v[64:65], v17 offset:22528\n\tglobal_load_lds_dwordx4 v5, s[98:99] offset:0\n\ts_waitcnt lgkmcnt(15)\n\tv_mfma_f32_16x16x32_bf16 v[108:111], v[84:87], a[80:83], v[108:111]\n\tds_read_b64_tr_b16 v[66:67], v17 offset:22784\n\tds_read_b64_tr_b16 v[68:69], v18 offset:4096\n\tds_read_b128 a[164:167], v29 offset:5120\n\ts_waitcnt lgkmcnt(15)\n\tv_mfma_f32_16x16x32_bf16 v[108:111], v[88:91], a[88:91], v[108:111]\n\tds_read_b64_tr_b16 v[70:71], v18 offset:4352\n\tds_read_b64_tr_b16 v[72:73], v18 offset:22528\n\tglobal_load_lds_dwordx4 v6, s[98:99] offset:1024\n\ts_waitcnt lgkmcnt(15)\n\tv_mfma_f32_16x16x32_bf16 v[108:111], v[44:47], a[96:99], v[108:111]\n\tds_read_b64_tr_b16 v[74:75], v18 offset:22784\n\tds_read_b64_tr_b16 v[76:77], v17 offset:5120\n\tds_read_b128 a[172:175], v30 offset:5120\n\ts_waitcnt lgkmcnt(15)\n\tv_mfma_f32_16x16x32_bf16 v[108:111], v[48:51], a[104:107], v[108:111]\n\tds_read_b64_tr_b16 v[78:79], v17 offset:5376\n\tds_read_b64_tr_b16 v[80:81], v17 offset:23552\n\ts_waitcnt lgkmcnt(15)\n\tv_mfma_f32_16x16x32_bf16 v[108:111], v[52:55], a[112:115], v[108:111]\n\tds_read_b64_tr_b16 v[82:83], v17 offset:23808\n\tds_read_b64_tr_b16 v[84:85], v18 offset:5120\n\tds_read_b128 a[180:183], v31 offset:5120\n\ts_waitcnt lgkmcnt(15)\n\tv_mfma_f32_16x16x32_bf16 v[108:111], v[56:59], a[120:123], v[108:111]\n\tds_read_b64_tr_b16 v[86:87], v18 offset:5376\n\tds_read_b64_tr_b16 v[88:89], v18 offset:23552\n\tglobal_load_lds_dwordx4 v7, s[98:99] offset:2048\n\ts_waitcnt lgkmcnt(15)\n\tv_mfma_f32_16x16x32_bf16 v[108:111], v[60:63], a[128:131], v[108:111]\n\tds_read_b64_tr_b16 v[90:91], v18 offset:23808\n\tds_read_b128 a[188:191], v32 offset:5120\n\ts_waitcnt lgkmcnt(15)\n\tv_mfma_f32_16x16x32_bf16 v[108:111], v[64:67], a[136:139], v[108:111]\n\ts_waitcnt vmcnt(6) lgkmcnt(1)\n\ts_barrier\n\tv_mfma_f32_16x16x32_bf16 v[108:111], v[68:71], a[144:147], v[108:111]\n\tds_read_b64_tr_b16 v[44:45], v17 offset:6144\n\tds_read_b64_tr_b16 v[48:49], v17 offset:24576\n\ts_add_u32 m0, s64, 0\n\tds_read_b128 a[196:199], v29 offset:6144\n\ts_add_u32 s98, s98, 0x6c000\n\ts_addc_u32 s99, s99, 0\n\tv_mfma_f32_16x16x32_bf16 v[108:111], v[72:75], a[152:155], v[108:111]\n\tds_read_b64_tr_b16 v[46:47], v17 offset:6400\n\tds_read_b64_tr_b16 v[50:51], v17 offset:24832\n\tds_read_b64_tr_b16 v[52:53], v18 offset:6144\n\tds_read_b128 a[204:207], v30 offset:6144\n\tglobal_load_lds_dwordx4 v5, s[98:99] offset:0\n\tv_mfma_f32_16x16x32_bf16 v[108:111], v[76:79], a[160:163], v[108:111]\n\tds_read_b64_tr_b16 v[54:55], v18 offset:6400\n\tds_read_b64_tr_b16 v[56:57], v18 offset:24576\n\tds_read_b128 a[212:215], v31 offset:6144\n\tv_mfma_f32_16x16x32_bf16 v[108:111], v[80:83], a[168:171], v[108:111]\n\tds_read_b64_tr_b16 v[58:59], v18 offset:24832\n\tds_read_b64_tr_b16 v[60:61], v17 offset:7168\n\tglobal_load_lds_dwordx4 v6, s[98:99] offset:1024\n\tv_mfma_f32_16x16x32_bf16 v[108:111], v[84:87], a[176:179], v[108:111]\n\tds_read_b64_tr_b16 v[62:63], v17 offset:7424\n\tds_read_b64_tr_b16 v[64:65], v17 offset:25600\n\tds_read_b128 a[220:223], v32 offset:6144\n\tds_read_b128 a[228:231], v29 offset:7168\n\tv_mfma_f32_16x16x32_bf16 v[108:111], v[88:91], a[184:187], v[108:111]\n\tds_read_b64_tr_b16 v[66:67], v17 offset:25856\n\tds_read_b64_tr_b16 v[68:69], v18 offset:7168\n\tglobal_load_lds_dwordx4 v7, s[98:99] offset:2048\n\ts_waitcnt lgkmcnt(14)\n\tv_mfma_f32_16x16x32_bf16 v[108:111], v[44:47], a[192:195], v[108:111]\n\tds_read_b64_tr_b16 v[70:71], v18 offset:7424\n\tds_read_b64_tr_b16 v[72:73], v18 offset:25600\n\tds_read_b64_tr_b16 v[76:77], v17 offset:8192\n\tds_read_b128 a[236:239], v30 offset:7168\n\ts_add_u32 m0, s64, 3072\n\tds_read_b128 a[244:247], v31 offset:7168\n\ts_waitcnt lgkmcnt(15)\n\tv_mfma_f32_16x16x32_bf16 v[108:111], v[48:51], a[200:203], v[108:111]\n\tds_read_b64_tr_b16 v[74:75], v18 offset:25856\n\tds_read_b64_tr_b16 v[78:79], v17 offset:8448\n\tds_read_b64_tr_b16 v[80:81], v17 offset:26624\n\ts_add_u32 s98, s98, 0x6c000\n\ts_addc_u32 s99, s99, 0\n\tds_read_b128 a[252:255], v32 offset:7168\n\tv_mfma_f32_16x16x32_bf16 v[108:111], v[52:55], a[208:211], v[108:111]\n\tds_read_b64_tr_b16 v[82:83], v17 offset:26880\n\tds_read_b64_tr_b16 v[84:85], v18 offset:8192\n\tglobal_load_lds_dwordx4 v5, s[98:99] offset:0\n\ts_waitcnt lgkmcnt(15)\n\tv_mfma_f32_16x16x32_bf16 v[108:111], v[56:59], a[216:219], v[108:111]\n\tds_read_b64_tr_b16 v[86:87], v18 offset:8448\n\tds_read_b64_tr_b16 v[88:89], v18 offset:26624\n\tds_read_b128 v[228:231], v29 offset:8192\n\tds_read_b128 v[236:239], v30 offset:8192\n\tv_mfma_f32_16x16x32_bf16 v[108:111], v[60:63], a[224:227], v[108:111]\n\tds_read_b64_tr_b16 v[90:91], v18 offset:26880\n\tglobal_load_lds_dwordx4 v6, s[98:99] offset:1024\n\tds_read_b128 v[244:247], v31 offset:8192\n\ts_waitcnt lgkmcnt(15)\n\tv_mfma_f32_16x16x32_bf16 v[108:111], v[64:67], a[232:235], v[108:111]\n\tglobal_load_lds_dwordx4 v7, s[98:99] offset:2048\n\tds_read_b128 v[252:255], v32 offset:8192\n\ts_waitcnt vmcnt(6) lgkmcnt(2)\n\ts_barrier\n\tv_mfma_f32_16x16x32_bf16 v[108:111], v[68:71], a[240:243], v[108:111]\n\tds_read_b64_tr_b16 v[44:45], v11 offset:12288\n\tds_read_b64_tr_b16 v[46:47], v11 offset:30720\n\ts_add_u32 m0, s64, 6144\n\ts_add_u32 s98, s98, 0x6c000\n\ts_addc_u32 s99, s99, 0\n\tds_read_b64_tr_b16 v[48:49], v11 offset:13312\n\tglobal_load_lds_dwordx4 v5, s[98:99] offset:0\n\tv_mfma_f32_16x16x32_bf16 v[108:111], v[72:75], a[248:251], v[108:111]\n\tds_read_b64_tr_b16 v[50:51], v11 offset:31744\n\tds_read_b64_tr_b16 v[52:53], v11 offset:14336\n\tglobal_load_lds_dwordx4 v6, s[98:99] offset:1024\n\tv_mfma_f32_16x16x32_bf16 v[108:111], v[76:79], v[224:227], v[108:111]\n\tds_read_b64_tr_b16 v[54:55], v11 offset:32768\n\tds_read_b64_tr_b16 v[56:57], v11 offset:15360\n\tds_read_b128 v[156:159], v33 offset:0\n\tds_read_b64_tr_b16 v[58:59], v11 offset:33792\n\tds_read_b64_tr_b16 v[60:61], v11 offset:16384\n\tglobal_load_lds_dwordx4 v7, s[98:99] offset:2048\n\tv_mfma_f32_16x16x32_bf16 v[108:111], v[80:83], v[232:235], v[108:111]\n\tds_read_b64_tr_b16 v[62:63], v11 offset:34816\n\tds_read_b64_tr_b16 v[64:65], v11 offset:17408\n\tds_read_b64_tr_b16 v[66:67], v11 offset:35840\n\tds_read_b128 v[160:163], v33 offset:32\n\ts_add_u32 m0, s64, 9216\n\tv_mfma_f32_16x16x32_bf16 v[108:111], v[84:87], v[240:243], v[108:111]\n\ts_add_u32 s98, s98, 0x6c000\n\ts_addc_u32 s99, s99, 0\n\tds_read_b128 v[164:167], v33 offset:64\n\tglobal_load_lds_dwordx4 v5, s[98:99] offset:0\n\tglobal_load_lds_dwordx4 v6, s[98:99] offset:1024\n\tv_mfma_f32_16x16x32_bf16 v[108:111], v[88:91], v[248:251], v[108:111]\n\tds_read_b128 v[168:171], v33 offset:96\n\tglobal_load_lds_dwordx4 v7, s[98:99] offset:2048\n\ts_waitcnt vmcnt(6) lgkmcnt(3)\n\ts_barrier\n\ts_waitcnt lgkmcnt(0)\n\tv_mfma_f32_32x32x16_bf16 v[124:139], v[44:47], a[0:3], v[156:171]\n\tds_read_b64_tr_b16 v[68:69], v11 offset:0\n\ts_add_u32 m0, s64, 12288\n\ts_add_u32 s98, s98, 0x6c000\n\ts_addc_u32 s99, s99, 0\n\tds_read_b64_tr_b16 v[70:71], v11 offset:18432\n\tv_mfma_f32_32x32x16_bf16 v[140:155], v[44:47], a[4:7], v[156:171]\n\tds_read_b64_tr_b16 v[72:73], v11 offset:1024\n\tglobal_load_lds_dwordx4 v5, s[98:99] offset:0\n\tv_and_b32 v220, 31, v0\n\ts_and_b32 s94, s62, 1\n\ts_mul_i32 s95, s94, 32\n\tv_add_u32 v220, s95, v220\n\tv_mul_u32_u24 v220, 200, v220\n\ts_lshr_b32 s94, s62, 1\n\ts_mul_i32 s95, s94, 64\n\tv_add_u32 v220, s95, v220\n\tv_lshrrev_b32 v223, 5, v0\n\tv_lshl_add_u32 v220, v223, 3, v220\n\tv_add_u32 v220, 150016, v220\n\tv_mfma_f32_32x32x16_bf16 v[124:139], v[48:51], a[8:11], v[124:139]\n\tds_read_b64_tr_b16 v[74:75], v11 offset:19456\n\tds_read_b64_tr_b16 v[76:77], v11 offset:2048\n\tglobal_load_lds_dwordx4 v6, s[98:99] offset:1024\n\tv_mfma_f32_32x32x16_bf16 v[140:155], v[48:51], a[12:15], v[140:155]\n\tds_read_b64_tr_b16 v[78:79], v11 offset:20480\n\tv_and_b32 v221, 15, v0\n\ts_and_b32 s93, s62, 1\n\ts_mul_i32 s94, s93, 32\n\ts_lshr_b32 s92, s62, 1\n\ts_mul_i32 s93, s92, 16\n\ts_add_u32 s95, s94, s93\n\tv_add_u32 v221, s95, v221\n\tv_mul_u32_u24 v221, 200, v221\n\tv_lshrrev_b32 v223, 4, v0\n\tv_lshl_add_u32 v221, v223, 3, v221\n\tv_add_u32 v221, 150144, v221\n\tv_mfma_f32_32x32x16_bf16 v[124:139], v[52:55], a[16:19], v[124:139]\n\tds_read_b64_tr_b16 v[80:81], v11 offset:3072\n\tglobal_load_lds_dwordx4 v7, s[98:99] offset:2048\n\tds_read_b64_tr_b16 v[82:83], v11 offset:21504\n\tv_mfma_f32_32x32x16_bf16 v[140:155], v[52:55], a[20:23], v[140:155]\n\tv_cvt_pk_bf16_f32 v188, v92, v93\n\tv_cvt_pk_bf16_f32 v189, v94, v95\n\tds_read_b64_tr_b16 v[84:85], v11 offset:4096\n\tds_write_b64 v220, v[188:189] offset:0\n\tv_mfma_f32_32x32x16_bf16 v[124:139], v[56:59], a[24:27], v[124:139]\n\tds_read_b64_tr_b16 v[86:87], v11 offset:22528\n\ts_add_u32 m0, s64, 15360\n\tv_cvt_pk_bf16_f32 v190, v96, v97\n\ts_add_u32 s98, s98, 0x6c000\n\ts_addc_u32 s99, s99, 0\n\tv_cvt_pk_bf16_f32 v191, v98, v99\n\tv_mfma_f32_32x32x16_bf16 v[140:155], v[56:59], a[28:31], v[140:155]\n\tds_read_b64_tr_b16 v[88:89], v11 offset:5120\n\tds_read_b64_tr_b16 v[90:91], v11 offset:23552\n\tglobal_load_lds_dwordx4 v5, s[98:99] offset:0\n\tds_write_b64 v220, v[190:191] offset:16\n\tv_mfma_f32_32x32x16_bf16 v[124:139], v[60:63], a[32:35], v[124:139]\n\tv_cvt_pk_bf16_f32 v192, v100, v101\n\tv_cvt_pk_bf16_f32 v193, v102, v103\n\tglobal_load_lds_dwordx4 v6, s[98:99] offset:1024\n\tv_mfma_f32_32x32x16_bf16 v[140:155], v[60:63], a[36:39], v[140:155]\n\tds_write_b64 v220, v[192:193] offset:32\n\tv_cvt_pk_bf16_f32 v194, v104, v105\n\tv_cvt_pk_bf16_f32 v195, v106, v107\n\tv_mfma_f32_32x32x16_bf16 v[124:139], v[64:67], a[40:43], v[124:139]\n\tds_write_b64 v220, v[194:195] offset:48\n\tglobal_load_lds_dwordx4 v7, s[98:99] offset:2048\n\tv_mfma_f32_32x32x16_bf16 v[140:155], v[64:67], a[44:47], v[140:155]\n\tv_cvt_pk_bf16_f32 v172, v108, v109\n\tv_cvt_pk_bf16_f32 v173, v110, v111\n\tds_write_b64 v221, v[172:173]\n\ts_waitcnt vmcnt(6) lgkmcnt(0)\n\ts_barrier\n\tv_mfma_f32_32x32x16_bf16 v[124:139], v[68:71], a[48:51], v[124:139]\n\tds_read_b64_tr_b16 v[44:45], v11 offset:6144\n\ts_add_u32 m0, s64, 0\n\ts_add_u32 s98, s98, 0x6c000\n\ts_addc_u32 s99, s99, 0\n\tds_read_b64_tr_b16 v[46:47], v11 offset:24576\n\tv_bfe_u32 v220, v0, 2, 2\n\tv_lshrrev_b32 v223, 4, v0\n\tv_lshl_add_u32 v220, v223, 3, v220\n\tv_mul_u32_u24 v220, 200, v220\n\tv_and_b32 v223, 3, v0\n\tv_lshl_add_u32 v220, v223, 3, v220\n\tv_add_u32 v220, 150016, v220\n\tv_mfma_f32_32x32x16_bf16 v[140:155], v[68:71], a[52:55], v[140:155]\n\tds_read_b64_tr_b16 v[48:49], v11 offset:7168\n\tglobal_load_lds_dwordx4 v5, s[98:99] offset:0\n\tv_mfma_f32_32x32x16_bf16 v[124:139], v[72:75], a[56:59], v[124:139]\n\tv_and_b32 v221, 15, v0\n\ts_mul_i32 s95, s62, 16\n\tv_add_u32 v221, s95, v221\n\tv_lshrrev_b32 v223, 4, v0\n\tv_lshlrev_b32 v223, 3, v223\n\tv_lshl_add_u32 v221, v221, 5, v223\n\tds_read_b64_tr_b16 v[50:51], v11 offset:25600\n\ts_mul_i32 s93, s61, 32\n\ts_add_u32 s94, s93, s60\n\ts_mul_i32 s95, s94, 10240\n\ts_add_u32 s79, s95, 18874368\n\tv_mfma_f32_32x32x16_bf16 v[140:155], v[72:75], a[60:63], v[140:155]\n\tds_read_b64_tr_b16 v[52:53], v11 offset:8192\n\tglobal_load_lds_dwordx4 v6, s[98:99] offset:1024\n\tds_read_b64_tr_b16 v[54:55], v11 offset:26624\n\tds_read_b64_tr_b16 v[92:93], v220 offset:0\n\tv_mfma_f32_32x32x16_bf16 v[124:139], v[76:79], a[64:67], v[124:139]\n\tds_read_b64_tr_b16 v[94:95], v220 offset:800\n\tds_read_b64_tr_b16 v[56:57], v11 offset:9216\n\tds_read_b64_tr_b16 v[96:97], v220 offset:6400\n\tds_read_b64_tr_b16 v[98:99], v220 offset:7200\n\tds_read_b64_tr_b16 v[100:101], v220 offset:32\n\tds_read_b64_tr_b16 v[102:103], v220 offset:832\n\tds_read_b64_tr_b16 v[104:105], v220 offset:6432\n\tglobal_load_lds_dwordx4 v7, s[98:99] offset:2048\n\tv_mfma_f32_32x32x16_bf16 v[140:155], v[76:79], a[68:71], v[140:155]\n\tds_read_b64_tr_b16 v[58:59], v11 offset:27648\n\tds_read_b64_tr_b16 v[106:107], v220 offset:7232\n\ts_waitcnt lgkmcnt(8)\n\tv_mfma_f32_16x16x32_bf16 v[108:111], v[92:95], v[196:199], 0\n\tds_read_b64_tr_b16 v[60:61], v11 offset:10240\n\tv_mfma_f32_32x32x16_bf16 v[124:139], v[80:83], a[72:75], v[124:139]\n\tv_mfma_f32_16x16x32_bf16 v[112:115], v[92:95], v[204:207], 0\n\tds_read_b64_tr_b16 v[62:63], v11 offset:28672\n\ts_add_u32 m0, s64, 3072\n\ts_waitcnt lgkmcnt(7)\n\tv_mfma_f32_16x16x32_bf16 v[108:111], v[96:99], v[200:203], v[108:111]\n\tv_mfma_f32_32x32x16_bf16 v[140:155], v[80:83], a[76:79], v[140:155]\n\tds_read_b64_tr_b16 v[64:65], v11 offset:11264\n\tds_read_b64_tr_b16 v[66:67], v11 offset:29696\n\ts_add_u32 s98, s98, 0x6c000\n\ts_addc_u32 s99, s99, 0\n\tv_mfma_f32_16x16x32_bf16 v[112:115], v[96:99], v[208:211], v[112:115]\n\tv_mfma_f32_32x32x16_bf16 v[124:139], v[84:87], a[80:83], v[124:139]\n\tglobal_load_lds_dwordx4 v5, s[98:99] offset:0\n\tds_read_b64_tr_b16 v[92:93], v220 offset:64\n\tds_read_b64_tr_b16 v[94:95], v220 offset:864\n\tds_read_b64_tr_b16 v[96:97], v220 offset:6464\n\tds_read_b64_tr_b16 v[98:99], v220 offset:7264\n\ts_waitcnt lgkmcnt(11)\n\tv_mfma_f32_16x16x32_bf16 v[116:119], v[100:103], v[196:199], 0\n\tv_mfma_f32_32x32x16_bf16 v[140:155], v[84:87], a[84:87], v[140:155]\n\tglobal_load_lds_dwordx4 v6, s[98:99] offset:1024\n\tv_mfma_f32_16x16x32_bf16 v[120:123], v[100:103], v[204:207], 0\n\tv_mfma_f32_32x32x16_bf16 v[124:139], v[88:91], a[88:91], v[124:139]\n\ts_waitcnt lgkmcnt(8)\n\tv_mfma_f32_16x16x32_bf16 v[116:119], v[104:107], v[200:203], v[116:119]\n\tglobal_load_lds_dwordx4 v7, s[98:99] offset:2048\n\tv_mfma_f32_32x32x16_bf16 v[140:155], v[88:91], a[92:95], v[140:155]\n\tv_mfma_f32_16x16x32_bf16 v[120:123], v[104:107], v[208:211], v[120:123]\n\tv_sub_f32 v108, v108, v113\n\tv_add_f32 v109, v109, v112\n\tv_sub_f32 v110, v110, v115\n\tv_add_f32 v111, v111, v114\n\ts_waitcnt vmcnt(6) lgkmcnt(4)\n\ts_barrier\n\tv_mfma_f32_32x32x16_bf16 v[124:139], v[44:47], a[96:99], v[124:139]\n\tds_read_b64_tr_b16 v[68:69], v11 offset:12288\n\ts_add_u32 m0, s64, 6144\n\tv_cvt_pk_bf16_f32 v172, v108, v109\n\tv_cvt_pk_bf16_f32 v173, v110, v111\n\ts_add_u32 s98, s98, 0x6c000\n\ts_addc_u32 s99, s99, 0\n\tbuffer_store_dwordx2 v[172:173], v221, s[56:59], s79 offen sc1\n\tv_mfma_f32_32x32x16_bf16 v[140:155], v[44:47], a[100:103], v[140:155]\n\tds_read_b64_tr_b16 v[70:71], v11 offset:30720\n\tds_read_b64_tr_b16 v[72:73], v11 offset:13312\n\tglobal_load_lds_dwordx4 v5, s[98:99] offset:0\n\tv_mfma_f32_32x32x16_bf16 v[124:139], v[48:51], a[104:107], v[124:139]\n\tds_read_b64_tr_b16 v[74:75], v11 offset:31744\n\ts_add_u32 s79, s79, 2048\n\tds_read_b64_tr_b16 v[100:101], v220 offset:96\n\tds_read_b64_tr_b16 v[102:103], v220 offset:896\n\tds_read_b64_tr_b16 v[104:105], v220 offset:6496\n\tds_read_b64_tr_b16 v[106:107], v220 offset:7296\n\tds_read_b64_tr_b16 v[76:77], v11 offset:14336\n\tv_mfma_f32_32x32x16_bf16 v[140:155], v[48:51], a[108:111], v[140:155]\n\ts_waitcnt lgkmcnt(11)\n\tv_mfma_f32_16x16x32_bf16 v[108:111], v[92:95], v[196:199], 0\n\tds_read_b64_tr_b16 v[78:79], v11 offset:32768\n\tglobal_load_lds_dwordx4 v6, s[98:99] offset:1024\n\tv_mfma_f32_32x32x16_bf16 v[124:139], v[52:55], a[112:115], v[124:139]\n\tds_read_b64_tr_b16 v[80:81], v11 offset:15360\n\tv_mfma_f32_16x16x32_bf16 v[112:115], v[92:95], v[204:207], 0\n\tds_read_b64_tr_b16 v[82:83], v11 offset:33792\n\ts_waitcnt lgkmcnt(12)\n\tv_mfma_f32_16x16x32_bf16 v[108:111], v[96:99], v[200:203], v[108:111]\n\tv_mfma_f32_32x32x16_bf16 v[140:155], v[52:55], a[116:119], v[140:155]\n\tglobal_load_lds_dwordx4 v7, s[98:99] offset:2048\n\tds_read_b64_tr_b16 v[84:85], v11 offset:16384\n\tv_mfma_f32_16x16x32_bf16 v[112:115], v[96:99], v[208:211], v[112:115]\n\tv_mfma_f32_32x32x16_bf16 v[124:139], v[56:59], a[120:123], v[124:139]\n\tds_read_b64_tr_b16 v[86:87], v11 offset:34816\n\tv_sub_f32 v116, v116, v121\n\ts_add_u32 m0, s64, 9216\n\tv_add_f32 v117, v117, v120\n\ts_add_u32 s98, s98, 0x6c000\n\ts_addc_u32 s99, s99, 0\n\tv_mfma_f32_32x32x16_bf16 v[140:155], v[56:59], a[124:127], v[140:155]\n\tds_read_b64_tr_b16 v[88:89], v11 offset:17408\n\tds_read_b64_tr_b16 v[90:91], v11 offset:35840\n\tv_sub_f32 v118, v118, v123\n\tv_add_f32 v119, v119, v122\n\tv_cvt_pk_bf16_f32 v174, v116, v117\n\tv_cvt_pk_bf16_f32 v175, v118, v119\n\tglobal_load_lds_dwordx4 v5, s[98:99] offset:0\n\tv_mfma_f32_32x32x16_bf16 v[124:139], v[60:63], a[128:131], v[124:139]\n\tbuffer_store_dwordx2 v[174:175], v221, s[56:59], s79 offen sc1\n\tv_mfma_f32_32x32x16_bf16 v[140:155], v[60:63], a[132:135], v[140:155]\n\ts_add_u32 s79, s79, 2048\n\tds_read_b64_tr_b16 v[92:93], v220 offset:128\n\tglobal_load_lds_dwordx4 v6, s[98:99] offset:1024\n\tds_read_b64_tr_b16 v[94:95], v220 offset:928\n\tds_read_b64_tr_b16 v[96:97], v220 offset:6528\n\tv_mfma_f32_32x32x16_bf16 v[124:139], v[64:67], a[136:139], v[124:139]\n\tds_read_b64_tr_b16 v[98:99], v220 offset:7328\n\ts_waitcnt lgkmcnt(14)\n\tv_mfma_f32_16x16x32_bf16 v[116:119], v[100:103], v[196:199], 0\n\tv_mfma_f32_16x16x32_bf16 v[120:123], v[100:103], v[204:207], 0\n\tv_mfma_f32_32x32x16_bf16 v[140:155], v[64:67], a[140:143], v[140:155]\n\tglobal_load_lds_dwordx4 v7, s[98:99] offset:2048\n\ts_waitcnt lgkmcnt(12)\n\tv_mfma_f32_16x16x32_bf16 v[116:119], v[104:107], v[200:203], v[116:119]\n\ts_waitcnt vmcnt(8) lgkmcnt(4)\n\ts_barrier\n\tv_mfma_f32_32x32x16_bf16 v[124:139], v[68:71], a[144:147], v[124:139]\n\tds_read_b64_tr_b16 v[44:45], v11 offset:0\n\ts_add_u32 m0, s64, 12288\n\tv_mfma_f32_16x16x32_bf16 v[120:123], v[104:107], v[208:211], v[120:123]\n\ts_sub_u32 s98, s98, 0x4a3f00\n\ts_subb_u32 s99, s99, 0\n\tv_mfma_f32_32x32x16_bf16 v[140:155], v[68:71], a[148:151], v[140:155]\n\tds_read_b64_tr_b16 v[46:47], v11 offset:18432\n\tv_sub_f32 v108, v108, v113\n\tds_read_b64_tr_b16 v[48:49], v11 offset:1024\n\tv_add_f32 v109, v109, v112\n\tv_sub_f32 v110, v110, v115\n\tv_add_f32 v111, v111, v114\n\tglobal_load_lds_dwordx4 v5, s[98:99] offset:0\n\tv_mfma_f32_32x32x16_bf16 v[124:139], v[72:75], a[152:155], v[124:139]\n\tds_read_b64_tr_b16 v[50:51], v11 offset:19456\n\tv_cvt_pk_bf16_f32 v172, v108, v109\n\tv_cvt_pk_bf16_f32 v173, v110, v111\n\tds_read_b64_tr_b16 v[52:53], v11 offset:2048\n\tbuffer_store_dwordx2 v[172:173], v221, s[56:59], s79 offen sc1\n\tv_mfma_f32_32x32x16_bf16 v[140:155], v[72:75], a[156:159], v[140:155]\n\tglobal_load_lds_dwordx4 v6, s[98:99] offset:1024\n\tds_read_b64_tr_b16 v[54:55], v11 offset:20480\n\ts_add_u32 s79, s79, 2048\n\tv_mfma_f32_32x32x16_bf16 v[124:139], v[76:79], a[160:163], v[124:139]\n\tds_read_b64_tr_b16 v[56:57], v11 offset:3072\n\ts_waitcnt lgkmcnt(9)\n\tv_mfma_f32_16x16x32_bf16 v[108:111], v[92:95], v[196:199], 0\n\tds_read_b64_tr_b16 v[58:59], v11 offset:21504\n\tv_mfma_f32_16x16x32_bf16 v[112:115], v[92:95], v[204:207], 0\n\tv_mfma_f32_32x32x16_bf16 v[140:155], v[76:79], a[164:167], v[140:155]\n\tglobal_load_lds_dwordx4 v7, s[98:99] offset:2048\n\tds_read_b64_tr_b16 v[60:61], v11 offset:4096\n\ts_waitcnt lgkmcnt(9)\n\tv_mfma_f32_16x16x32_bf16 v[108:111], v[96:99], v[200:203], v[108:111]\n\tv_mfma_f32_32x32x16_bf16 v[124:139], v[80:83], a[168:171], v[124:139]\n\tds_read_b64_tr_b16 v[62:63], v11 offset:22528\n\tv_mfma_f32_16x16x32_bf16 v[112:115], v[96:99], v[208:211], v[112:115]\n\ts_add_u32 m0, s64, 15360\n\tv_mfma_f32_32x32x16_bf16 v[140:155], v[80:83], a[172:175], v[140:155]\n\tds_read_b64_tr_b16 v[64:65], v11 offset:5120\n\tds_read_b64_tr_b16 v[66:67], v11 offset:23552\n\ts_add_u32 s98, s98, 0x6c000\n\ts_addc_u32 s99, s99, 0\n\tv_sub_f32 v116, v116, v121\n\tv_add_f32 v117, v117, v120\n\tglobal_load_lds_dwordx4 v5, s[98:99] offset:0\n\tv_mfma_f32_32x32x16_bf16 v[124:139], v[84:87], a[176:179], v[124:139]\n\tv_sub_f32 v118, v118, v123\n\tv_add_f32 v119, v119, v122\n\tv_cvt_pk_bf16_f32 v174, v116, v117\n\tv_cvt_pk_bf16_f32 v175, v118, v119\n\tbuffer_store_dwordx2 v[174:175], v221, s[56:59], s79 offen sc1\n\tv_mfma_f32_32x32x16_bf16 v[140:155], v[84:87], a[180:183], v[140:155]\n\tglobal_load_lds_dwordx4 v6, s[98:99] offset:1024\n\ts_add_u32 s79, s79, 2048\n\tv_sub_f32 v108, v108, v113\n\tv_mfma_f32_32x32x16_bf16 v[124:139], v[88:91], a[184:187], v[124:139]\n\tv_add_f32 v109, v109, v112\n\tv_sub_f32 v110, v110, v115\n\tv_add_f32 v111, v111, v114\n\tv_cvt_pk_bf16_f32 v172, v108, v109\n\tv_cvt_pk_bf16_f32 v173, v110, v111\n\tglobal_load_lds_dwordx4 v7, s[98:99] offset:2048\n\tv_mfma_f32_32x32x16_bf16 v[140:155], v[88:91], a[188:191], v[140:155]\n\tbuffer_store_dwordx2 v[172:173], v221, s[56:59], s79 offen sc1\n\ts_waitcnt vmcnt(9) lgkmcnt(0)\n\ts_barrier\n\tv_mfma_f32_32x32x16_bf16 v[124:139], v[44:47], a[192:195], v[124:139]\n\tds_read_b64_tr_b16 v[68:69], v11 offset:6144\n\ts_add_u32 m0, s64, 0\n\ts_add_u32 s98, s98, 0x6c000\n\ts_addc_u32 s99, s99, 0\n\tds_read_b64_tr_b16 v[70:71], v11 offset:24576\n\tv_mfma_f32_32x32x16_bf16 v[140:155], v[44:47], a[196:199], v[140:155]\n\tds_read_b64_tr_b16 v[72:73], v11 offset:7168\n\tglobal_load_lds_dwordx4 v5, s[98:99] offset:0\n\tv_mfma_f32_32x32x16_bf16 v[124:139], v[48:51], a[200:203], v[124:139]\n\tds_read_b64_tr_b16 v[74:75], v11 offset:25600\n\tds_read_b64_tr_b16 v[76:77], v11 offset:8192\n\tglobal_load_lds_dwordx4 v6, s[98:99] offset:1024\n\tv_mfma_f32_32x32x16_bf16 v[140:155], v[48:51], a[204:207], v[140:155]\n\tds_read_b64_tr_b16 v[78:79], v11 offset:26624\n\tv_mfma_f32_32x32x16_bf16 v[124:139], v[52:55], a[208:211], v[124:139]\n\tds_read_b64_tr_b16 v[80:81], v11 offset:9216\n\tglobal_load_lds_dwordx4 v7, s[98:99] offset:2048\n\tds_read_b64_tr_b16 v[82:83], v11 offset:27648\n\tv_mfma_f32_32x32x16_bf16 v[140:155], v[52:55], a[212:215], v[140:155]\n\tds_read_b64_tr_b16 v[84:85], v11 offset:10240\n\tv_mfma_f32_32x32x16_bf16 v[124:139], v[56:59], a[216:219], v[124:139]\n\tds_read_b64_tr_b16 v[86:87], v11 offset:28672\n\ts_add_u32 m0, s64, 3072\n\ts_add_u32 s98, s98, 0x6c000\n\ts_addc_u32 s99, s99, 0\n\tds_read_b64_tr_b16 v[88:89], v11 offset:11264\n\tv_mfma_f32_32x32x16_bf16 v[140:155], v[56:59], a[220:223], v[140:155]\n\tds_read_b64_tr_b16 v[90:91], v11 offset:29696\n\tglobal_load_lds_dwordx4 v5, s[98:99] offset:0\n\tv_mfma_f32_32x32x16_bf16 v[124:139], v[60:63], a[224:227], v[124:139]\n\tglobal_load_lds_dwordx4 v6, s[98:99] offset:1024\n\tv_mfma_f32_32x32x16_bf16 v[140:155], v[60:63], a[228:231], v[140:155]\n\tv_mfma_f32_32x32x16_bf16 v[124:139], v[64:67], a[232:235], v[124:139]\n\tglobal_load_lds_dwordx4 v7, s[98:99] offset:2048\n\tv_mfma_f32_32x32x16_bf16 v[140:155], v[64:67], a[236:239], v[140:155]\n\ts_waitcnt vmcnt(7) lgkmcnt(0)\n\ts_barrier\n\tv_mfma_f32_32x32x16_bf16 v[124:139], v[68:71], a[240:243], v[124:139]\n\tds_read_b64_tr_b16 v[44:45], v11 offset:12288\n\ts_add_u32 m0, s64, 6144\n\ts_add_u32 s98, s98, 0x6c000\n\ts_addc_u32 s99, s99, 0\n\tds_read_b64_tr_b16 v[46:47], v11 offset:30720\n\tv_mfma_f32_32x32x16_bf16 v[140:155], v[68:71], a[244:247], v[140:155]\n\tds_read_b64_tr_b16 v[48:49], v11 offset:13312\n\tglobal_load_lds_dwordx4 v5, s[98:99] offset:0\n\tv_mfma_f32_32x32x16_bf16 v[124:139], v[72:75], a[248:251], v[124:139]\n\tds_read_b64_tr_b16 v[50:51], v11 offset:31744\n\tds_read_b128 v[156:159], v33 offset:512\n\tds_read_b64_tr_b16 v[52:53], v11 offset:14336\n\tv_mfma_f32_32x32x16_bf16 v[140:155], v[72:75], a[252:255], v[140:155]\n\tglobal_load_lds_dwordx4 v6, s[98:99] offset:1024\n\tds_read_b64_tr_b16 v[54:55], v11 offset:32768\n\tv_mfma_f32_32x32x16_bf16 v[124:139], v[76:79], v[224:227], v[124:139]\n\tds_read_b64_tr_b16 v[56:57], v11 offset:15360\n\tglobal_load_lds_dwordx4 v7, s[98:99] offset:2048\n\tds_read_b128 v[160:163], v33 offset:544\n\tv_mfma_f32_32x32x16_bf16 v[140:155], v[76:79], v[228:231], v[140:155]\n\tds_read_b64_tr_b16 v[58:59], v11 offset:33792\n\tds_read_b64_tr_b16 v[60:61], v11 offset:16384\n\tv_mfma_f32_32x32x16_bf16 v[124:139], v[80:83], v[232:235], v[124:139]\n\tds_read_b64_tr_b16 v[62:63], v11 offset:34816\n\ts_add_u32 m0, s64, 9216\n\ts_add_u32 s98, s98, 0x6c000\n\ts_addc_u32 s99, s99, 0\n\tds_read_b64_tr_b16 v[64:65], v11 offset:17408\n\tv_mfma_f32_32x32x16_bf16 v[140:155], v[80:83], v[236:239], v[140:155]\n\tds_read_b64_tr_b16 v[66:67], v11 offset:35840\n\tds_read_b128 v[164:167], v33 offset:576\n\tglobal_load_lds_dwordx4 v5, s[98:99] offset:0\n\tv_mfma_f32_32x32x16_bf16 v[124:139], v[84:87], v[240:243], v[124:139]\n\tglobal_load_lds_dwordx4 v6, s[98:99] offset:1024\n\tv_mfma_f32_32x32x16_bf16 v[140:155], v[84:87], v[244:247], v[140:155]\n\tds_read_b128 v[168:171], v33 offset:608\n\tv_mfma_f32_32x32x16_bf16 v[124:139], v[88:91], v[248:251], v[124:139]\n\tglobal_load_lds_dwordx4 v7, s[98:99] offset:2048\n\tv_mfma_f32_32x32x16_bf16 v[140:155], v[88:91], v[252:255], v[140:155]\n\ts_waitcnt vmcnt(6) lgkmcnt(2)\n\ts_barrier\n\ts_mul_i32 s93, s61, 256\n\ts_mul_i32 s92, s60, 8\n\ts_add_u32 s94, s93, s92\n\ts_add_u32 s95, s94, 0\n\ts_add_u32 s83, s95, 24117248\n\tv_mov_b32 v222, s88\n\tv_mov_b32 v223, s89\n\tbuffer_store_dwordx2 v[222:223], v42, s[56:59], s83 offen sc1\n\ts_waitcnt lgkmcnt(0)\n\tv_mfma_f32_32x32x16_bf16 v[92:107], v[44:47], a[0:3], v[156:171]\n\tds_read_b64_tr_b16 v[68:69], v11 offset:0\n\ts_add_u32 m0, s64, 12288\n\ts_add_u32 s98, s98, 0x6c000\n\ts_addc_u32 s99, s99, 0\n\tds_read_b64_tr_b16 v[70:71], v11 offset:18432\n\tv_mfma_f32_32x32x16_bf16 v[108:123], v[44:47], a[4:7], v[156:171]\n\tv_exp_f32 v172, v124\n\tds_read_b64_tr_b16 v[72:73], v11 offset:1024\n\tv_exp_f32 v173, v125\n\tglobal_load_lds_dwordx4 v5, s[98:99] offset:0\n\tv_mfma_f32_32x32x16_bf16 v[92:107], v[48:51], a[8:11], v[92:107]\n\tds_read_b64_tr_b16 v[74:75], v11 offset:19456\n\tv_fma_f32 v172, v172, s69, s69\n\tv_fma_f32 v172, v172, v172, 0.5\n\tv_exp_f32 v174, v126\n\tds_read_b64_tr_b16 v[76:77], v11 offset:2048\n\tglobal_load_lds_dwordx4 v6, s[98:99] offset:1024\n\tv_mfma_f32_32x32x16_bf16 v[108:123], v[48:51], a[12:15], v[108:123]\n\tv_fma_f32 v173, v173, s69, s69\n\tds_read_b64_tr_b16 v[78:79], v11 offset:20480\n\tv_rcp_f32 v172, v172\n\tv_fma_f32 v173, v173, v173, 0.5\n\tv_mfma_f32_32x32x16_bf16 v[92:107], v[52:55], a[16:19], v[92:107]\n\tds_read_b64_tr_b16 v[80:81], v11 offset:3072\n\tv_exp_f32 v175, v127\n\tds_read_b64_tr_b16 v[82:83], v11 offset:21504\n\tv_fma_f32 v174, v174, s69, s69\n\tglobal_load_lds_dwordx4 v7, s[98:99] offset:2048\n\tv_mfma_f32_32x32x16_bf16 v[108:123], v[52:55], a[20:23], v[108:123]\n\tv_rcp_f32 v173, v173\n\tds_read_b64_tr_b16 v[84:85], v11 offset:4096\n\tv_fma_f32 v174, v174, v174, 0.5\n\tv_fma_f32 v172, -v124, v172, v124\n\tv_mfma_f32_32x32x16_bf16 v[92:107], v[56:59], a[24:27], v[92:107]\n\tds_read_b64_tr_b16 v[86:87], v11 offset:22528\n\tv_exp_f32 v176, v128\n\ts_add_u32 m0, s64, 15360\n\tv_fma_f32 v175, v175, s69, s69\n\tds_read_b64_tr_b16 v[88:89], v11 offset:5120\n\tv_mfma_f32_32x32x16_bf16 v[108:123], v[56:59], a[28:31], v[108:123]\n\tds_read_b64_tr_b16 v[90:91], v11 offset:23552\n\ts_add_u32 s98, s98, 0x6c000\n\ts_addc_u32 s99, s99, 0\n\tv_rcp_f32 v174, v174\n\tv_fma_f32 v175, v175, v175, 0.5\n\tglobal_load_lds_dwordx4 v5, s[98:99] offset:0\n\tv_mfma_f32_32x32x16_bf16 v[92:107], v[60:63], a[32:35], v[92:107]\n\tv_fma_f32 v173, -v125, v173, v125\n\tv_exp_f32 v177, v129\n\tv_fma_f32 v176, v176, s69, s69\n\tv_rcp_f32 v175, v175\n\tv_mfma_f32_32x32x16_bf16 v[108:123], v[60:63], a[36:39], v[108:123]\n\tglobal_load_lds_dwordx4 v6, s[98:99] offset:1024\n\tv_fma_f32 v176, v176, v176, 0.5\n\tv_fma_f32 v174, -v126, v174, v126\n\tv_mfma_f32_32x32x16_bf16 v[92:107], v[64:67], a[40:43], v[92:107]\n\tv_cvt_pk_bf16_f32 v188, v172, v173\n\tv_exp_f32 v178, v130\n\tv_fma_f32 v177, v177, s69, s69\n\tv_rcp_f32 v176, v176\n\tv_mfma_f32_32x32x16_bf16 v[108:123], v[64:67], a[44:47], v[108:123]\n\tglobal_load_lds_dwordx4 v7, s[98:99] offset:2048\n\tv_fma_f32 v177, v177, v177, 0.5\n\tv_fma_f32 v175, -v127, v175, v127\n\tv_exp_f32 v179, v131\n\ts_waitcnt vmcnt(7) lgkmcnt(0)\n\ts_barrier\n\tv_mfma_f32_32x32x16_bf16 v[92:107], v[68:71], a[48:51], v[92:107]\n\tds_read_b64_tr_b16 v[44:45], v11 offset:6144\n\ts_add_u32 m0, s64, 0\n\tv_fma_f32 v178, v178, s69, s69\n\tv_rcp_f32 v177, v177\n\tds_read_b64_tr_b16 v[46:47], v11 offset:24576\n\tv_mfma_f32_32x32x16_bf16 v[108:123], v[68:71], a[52:55], v[108:123]\n\ts_add_u32 s98, s98, 0x6c000\n\ts_addc_u32 s99, s99, 0\n\tv_fma_f32 v178, v178, v178, 0.5\n\tds_read_b64_tr_b16 v[48:49], v11 offset:7168\n\tv_fma_f32 v176, -v128, v176, v128\n\tv_cvt_pk_bf16_f32 v189, v174, v175\n\tglobal_load_lds_dwordx4 v5, s[98:99] offset:0\n\tv_mfma_f32_32x32x16_bf16 v[92:107], v[72:75], a[56:59], v[92:107]\n\tds_read_b64_tr_b16 v[50:51], v11 offset:25600\n\tv_exp_f32 v180, v132\n\tv_fma_f32 v179, v179, s69, s69\n\tds_read_b64_tr_b16 v[52:53], v11 offset:8192\n\tv_mfma_f32_32x32x16_bf16 v[108:123], v[72:75], a[60:63], v[108:123]\n\tv_rcp_f32 v178, v178\n\tglobal_load_lds_dwordx4 v6, s[98:99] offset:1024\n\tv_mfma_f32_32x32x16_bf16 v[92:107], v[76:79], a[64:67], v[92:107]\n\tds_read_b64_tr_b16 v[54:55], v11 offset:26624\n\tv_fma_f32 v179, v179, v179, 0.5\n\tds_read_b64_tr_b16 v[56:57], v11 offset:9216\n\tv_fma_f32 v177, -v129, v177, v129\n\tv_exp_f32 v181, v133\n\tv_mfma_f32_32x32x16_bf16 v[108:123], v[76:79], a[68:71], v[108:123]\n\tds_read_b64_tr_b16 v[58:59], v11 offset:27648\n\tglobal_load_lds_dwordx4 v7, s[98:99] offset:2048\n\tv_fma_f32 v180, v180, s69, s69\n\tv_rcp_f32 v179, v179\n\tv_mfma_f32_32x32x16_bf16 v[92:107], v[80:83], a[72:75], v[92:107]\n\tds_read_b64_tr_b16 v[60:61], v11 offset:10240\n\tds_read_b64_tr_b16 v[62:63], v11 offset:28672\n\tv_fma_f32 v180, v180, v180, 0.5\n\tv_fma_f32 v178, -v130, v178, v130\n\ts_add_u32 m0, s64, 3072\n\tv_cvt_pk_bf16_f32 v190, v176, v177\n\tv_mfma_f32_32x32x16_bf16 v[108:123], v[80:83], a[76:79], v[108:123]\n\tds_read_b64_tr_b16 v[64:65], v11 offset:11264\n\tds_read_b64_tr_b16 v[66:67], v11 offset:29696\n\ts_add_u32 s98, s98, 0x6c000\n\ts_addc_u32 s99, s99, 0\n\tv_exp_f32 v182, v134\n\tv_fma_f32 v181, v181, s69, s69\n\tv_mfma_f32_32x32x16_bf16 v[92:107], v[84:87], a[80:83], v[92:107]\n\tglobal_load_lds_dwordx4 v5, s[98:99] offset:0\n\tv_rcp_f32 v180, v180\n\tv_mfma_f32_32x32x16_bf16 v[108:123], v[84:87], a[84:87], v[108:123]\n\tv_fma_f32 v181, v181, v181, 0.5\n\tv_fma_f32 v179, -v131, v179, v131\n\tv_exp_f32 v183, v135\n\tv_fma_f32 v182, v182, s69, s69\n\tglobal_load_lds_dwordx4 v6, s[98:99] offset:1024\n\tv_mfma_f32_32x32x16_bf16 v[92:107], v[88:91], a[88:91], v[92:107]\n\tv_rcp_f32 v181, v181\n\tv_fma_f32 v182, v182, v182, 0.5\n\tv_fma_f32 v180, -v132, v180, v132\n\tv_cvt_pk_bf16_f32 v191, v178, v179\n\tv_mfma_f32_32x32x16_bf16 v[108:123], v[88:91], a[92:95], v[108:123]\n\tglobal_load_lds_dwordx4 v7, s[98:99] offset:2048\n\tv_exp_f32 v184, v136\n\tv_fma_f32 v183, v183, s69, s69\n\tds_write_b128 v19, v[188:191] offset:0\n\ts_waitcnt vmcnt(6) lgkmcnt(1)\n\ts_barrier\n\tv_mfma_f32_32x32x16_bf16 v[92:107], v[44:47], a[96:99], v[92:107]\n\tds_read_b64_tr_b16 v[68:69], v11 offset:12288\n\ts_add_u32 m0, s64, 6144\n\tv_rcp_f32 v182, v182\n\ts_add_u32 s98, s98, 0x6c000\n\ts_addc_u32 s99, s99, 0\n\tds_read_b64_tr_b16 v[70:71], v11 offset:30720\n\tv_mfma_f32_32x32x16_bf16 v[108:123], v[44:47], a[100:103], v[108:123]\n\ts_mul_i32 s94, s61, 256\n\ts_add_u32 s95, s94, 0\n\ts_add_u32 s78, s95, 24117248\n\tv_fma_f32 v183, v183, v183, 0.5\n\tv_fma_f32 v181, -v133, v181, v133\n\tds_read_b64_tr_b16 v[72:73], v11 offset:13312\n\tv_exp_f32 v185, v137\n\tv_mfma_f32_32x32x16_bf16 v[92:107], v[48:51], a[104:107], v[92:107]\n\tv_and_b32 v210, 31, v0\n\tglobal_load_lds_dwordx4 v5, s[98:99] offset:0\n\tds_read_b64_tr_b16 v[74:75], v11 offset:31744\n\tv_fma_f32 v184, v184, s69, s69\n\tv_rcp_f32 v183, v183\n\tv_mfma_f32_32x32x16_bf16 v[108:123], v[48:51], a[108:111], v[108:123]\n\tv_lshlrev_b32 v210, 3, v210\n\tbuffer_load_dwordx2 v[208:209], v210, s[56:59], s78 offen sc1\n\tds_read_b64_tr_b16 v[76:77], v11 offset:14336\n\tv_mfma_f32_32x32x16_bf16 v[92:107], v[52:55], a[112:115], v[92:107]\n\tds_read_b64_tr_b16 v[78:79], v11 offset:32768\n\tv_fma_f32 v184, v184, v184, 0.5\n\tglobal_load_lds_dwordx4 v6, s[98:99] offset:1024\n\tv_fma_f32 v182, -v134, v182, v134\n\tv_cvt_pk_bf16_f32 v192, v180, v181\n\tv_mfma_f32_32x32x16_bf16 v[108:123], v[52:55], a[116:119], v[108:123]\n\tds_read_b64_tr_b16 v[80:81], v11 offset:15360\n\tv_exp_f32 v186, v138\n\tds_read_b64_tr_b16 v[82:83], v11 offset:33792\n\tv_fma_f32 v185, v185, s69, s69\n\tds_read_b64_tr_b16 v[84:85], v11 offset:16384\n\tv_rcp_f32 v184, v184\n\tv_mfma_f32_32x32x16_bf16 v[92:107], v[56:59], a[120:123], v[92:107]\n\tglobal_load_lds_dwordx4 v7, s[98:99] offset:2048\n\tv_fma_f32 v185, v185, v185, 0.5\n\tv_fma_f32 v183, -v135, v183, v135\n\tds_read_b64_tr_b16 v[86:87], v11 offset:34816\n\tv_mfma_f32_32x32x16_bf16 v[108:123], v[56:59], a[124:127], v[108:123]\n\tds_read_b64_tr_b16 v[88:89], v11 offset:17408\n\tds_read_b64_tr_b16 v[90:91], v11 offset:35840\n\tv_exp_f32 v187, v139\n\ts_add_u32 m0, s64, 9216\n\tv_fma_f32 v186, v186, s69, s69\n\ts_add_u32 s98, s98, 0x6c000\n\ts_addc_u32 s99, s99, 0\n\tv_mfma_f32_32x32x16_bf16 v[92:107], v[60:63], a[128:131], v[92:107]\n\tv_rcp_f32 v185, v185\n\tv_fma_f32 v186, v186, v186, 0.5\n\tv_fma_f32 v184, -v136, v184, v136\n\tv_cvt_pk_bf16_f32 v193, v182, v183\n\tglobal_load_lds_dwordx4 v5, s[98:99] offset:0\n\tv_mfma_f32_32x32x16_bf16 v[108:123], v[60:63], a[132:135], v[108:123]\n\tv_exp_f32 v172, v140\n\tv_fma_f32 v187, v187, s69, s69\n\tv_rcp_f32 v186, v186\n\tv_fma_f32 v187, v187, v187, 0.5\n\tv_mfma_f32_32x32x16_bf16 v[92:107], v[64:67], a[136:139], v[92:107]\n\tglobal_load_lds_dwordx4 v6, s[98:99] offset:1024\n\tv_fma_f32 v185, -v137, v185, v137\n\tv_exp_f32 v173, v141\n\tglobal_load_lds_dwordx4 v7, s[98:99] offset:2048\n\tv_mfma_f32_32x32x16_bf16 v[108:123], v[64:67], a[140:143], v[108:123]\n\tv_fma_f32 v172, v172, s69, s69\n\tv_rcp_f32 v187, v187\n\tv_fma_f32 v172, v172, v172, 0.5\n\tv_fma_f32 v186, -v138, v186, v138\n\ts_waitcnt vmcnt(7) lgkmcnt(0)\n\ts_barrier\n\tv_mfma_f32_32x32x16_bf16 v[92:107], v[68:71], a[144:147], v[92:107]\n\tds_read_b64_tr_b16 v[44:45], v11 offset:0\n\tv_cvt_pk_bf16_f32 v194, v184, v185\n\ts_add_u32 m0, s64, 12288\n\tv_exp_f32 v174, v142\n\tds_read_b64_tr_b16 v[46:47], v11 offset:18432\n\tv_mfma_f32_32x32x16_bf16 v[108:123], v[68:71], a[148:151], v[108:123]\n\ts_sub_u32 s98, s98, 0x4a3f00\n\ts_subb_u32 s99, s99, 0\n\tv_fma_f32 v173, v173, s69, s69\n\tds_read_b64_tr_b16 v[48:49], v11 offset:1024\n\tv_rcp_f32 v172, v172\n\tglobal_load_lds_dwordx4 v5, s[98:99] offset:0\n\tv_mfma_f32_32x32x16_bf16 v[92:107], v[72:75], a[152:155], v[92:107]\n\tds_read_b64_tr_b16 v[50:51], v11 offset:19456\n\tv_fma_f32 v173, v173, v173, 0.5\n\tv_fma_f32 v187, -v139, v187, v139\n\tv_exp_f32 v175, v143\n\tds_read_b64_tr_b16 v[52:53], v11 offset:2048\n\tv_mfma_f32_32x32x16_bf16 v[108:123], v[72:75], a[156:159], v[108:123]\n\tglobal_load_lds_dwordx4 v6, s[98:99] offset:1024\n\tv_fma_f32 v174, v174, s69, s69\n\tds_read_b64_tr_b16 v[54:55], v11 offset:20480\n\tv_mfma_f32_32x32x16_bf16 v[92:107], v[76:79], a[160:163], v[92:107]\n\tv_rcp_f32 v173, v173\n\tds_read_b64_tr_b16 v[56:57], v11 offset:3072\n\tv_fma_f32 v174, v174, v174, 0.5\n\tv_fma_f32 v172, -v140, v172, v140\n\tv_cvt_pk_bf16_f32 v195, v186, v187\n\tv_mfma_f32_32x32x16_bf16 v[108:123], v[76:79], a[164:167], v[108:123]\n\tds_read_b64_tr_b16 v[58:59], v11 offset:21504\n\tglobal_load_lds_dwordx4 v7, s[98:99] offset:2048\n\tds_read_b64_tr_b16 v[60:61], v11 offset:4096\n\tv_exp_f32 v176, v144\n\tv_mfma_f32_32x32x16_bf16 v[92:107], v[80:83], a[168:171], v[92:107]\n\tv_fma_f32 v175, v175, s69, s69\n\tds_read_b64_tr_b16 v[62:63], v11 offset:22528\n\tv_rcp_f32 v174, v174\n\ts_add_u32 m0, s64, 15360\n\tv_fma_f32 v175, v175, v175, 0.5\n\tv_mfma_f32_32x32x16_bf16 v[108:123], v[80:83], a[172:175], v[108:123]\n\tds_read_b64_tr_b16 v[64:65], v11 offset:5120\n\tds_read_b64_tr_b16 v[66:67], v11 offset:23552\n\ts_add_u32 s98, s98, 0x6c000\n\ts_addc_u32 s99, s99, 0\n\tv_fma_f32 v173, -v141, v173, v141\n\tv_exp_f32 v177, v145\n\tv_mfma_f32_32x32x16_bf16 v[92:107], v[84:87], a[176:179], v[92:107]\n\tglobal_load_lds_dwordx4 v5, s[98:99] offset:0\n\tv_fma_f32 v176, v176, s69, s69\n\tv_rcp_f32 v175, v175\n\tv_mfma_f32_32x32x16_bf16 v[108:123], v[84:87], a[180:183], v[108:123]\n\tv_fma_f32 v176, v176, v176, 0.5\n\tv_fma_f32 v174, -v142, v174, v142\n\tglobal_load_lds_dwordx4 v6, s[98:99] offset:1024\n\tv_mfma_f32_32x32x16_bf16 v[92:107], v[88:91], a[184:187], v[92:107]\n\tv_cvt_pk_bf16_f32 v188, v172, v173\n\tv_exp_f32 v178, v146\n\tv_fma_f32 v177, v177, s69, s69\n\tv_rcp_f32 v176, v176\n\tglobal_load_lds_dwordx4 v7, s[98:99] offset:2048\n\tv_mfma_f32_32x32x16_bf16 v[108:123], v[88:91], a[188:191], v[108:123]\n\tv_fma_f32 v177, v177, v177, 0.5\n\tv_fma_f32 v175, -v143, v175, v143\n\tv_exp_f32 v179, v147\n\tds_write_b128 v20, v[192:195] offset:0\n\ts_waitcnt vmcnt(6) lgkmcnt(1)\n\ts_barrier\n\tv_cmp_ne_u64 vcc, s[88:89], v[208:209]\n\ts_nop 3\n\ts_cbranch_vccz L_ok_y_%=\n\ts_mov_b32 s83, 0\n\tL_sp_y_%=:\n\ts_sleep 1\n\tbuffer_load_dwordx2 v[208:209], v210, s[56:59], s78 offen sc1\n\ts_waitcnt vmcnt(0)\n\tv_cmp_ne_u64 vcc, s[88:89], v[208:209]\n\ts_add_u32 s83, s83, 1\n\ts_nop 2\n\ts_cbranch_vccz L_ok_y_%=\n\ts_cmp_lt_u32 s83, 16384\n\ts_cbranch_scc1 L_sp_y_%=\n\tL_ok_y_%=:\n\tv_mfma_f32_32x32x16_bf16 v[92:107], v[44:47], a[192:195], v[92:107]\n\tds_read_b64_tr_b16 v[68:69], v11 offset:6144\n\ts_add_u32 m0, s64, 0\n\tv_fma_f32 v178, v178, s69, s69\n\tv_rcp_f32 v177, v177\n\tds_read_b64_tr_b16 v[70:71], v11 offset:24576\n\ts_and_b32 s94, s62, 1\n\ts_mul_i32 s95, s94, 64\n\tv_add_u32 v196, s95, v0\n\tv_lshrrev_b32 v196, 2, v196\n\tv_mul_u32_u24 v196, 10240, v196\n\tv_and_b32 v222, 3, v0\n\tv_lshl_add_u32 v196, v222, 4, v196\n\ts_add_u32 s98, s98, 0x6c000\n\ts_addc_u32 s99, s99, 0\n\tv_mfma_f32_32x32x16_bf16 v[108:123], v[44:47], a[196:199], v[108:123]\n\tv_fma_f32 v178, v178, v178, 0.5\n\tds_read_b64_tr_b16 v[72:73], v11 offset:7168\n\tglobal_load_lds_dwordx4 v5, s[98:99] offset:0\n\tv_fma_f32 v176, -v144, v176, v144\n\tv_cvt_pk_bf16_f32 v189, v174, v175\n\ts_and_b32 s94, s62, 1\n\ts_mul_i32 s95, s94, 64\n\tv_add_u32 v197, s95, v0\n\tv_lshrrev_b32 v197, 2, v197\n\tv_mul_u32_u24 v197, 336, v197\n\tv_bfe_u32 v222, v0, 1, 1\n\tv_mul_u32_u24 v222, 160, v222\n\tv_add_u32 v197, v197, v222\n\tv_and_b32 v222, 1, v0\n\tv_lshl_add_u32 v197, v222, 4, v197\n\ts_lshr_b32 s93, s62, 1\n\ts_mul_i32 s94, s93, 32\n\ts_add_u32 s95, s94, 150016\n\tv_add_u32 v197, s95, v197\n\tv_mfma_f32_32x32x16_bf16 v[92:107], v[48:51], a[200:203], v[92:107]\n\tds_read_b64_tr_b16 v[74:75], v11 offset:25600\n\tv_exp_f32 v180, v148\n\tv_fma_f32 v179, v179, s69, s69\n\tds_read_b64_tr_b16 v[76:77], v11 offset:8192\n\tv_rcp_f32 v178, v178\n\tglobal_load_lds_dwordx4 v6, s[98:99] offset:1024\n\tv_mfma_f32_32x32x16_bf16 v[108:123], v[48:51], a[204:207], v[108:123]\n\tds_read_b64_tr_b16 v[78:79], v11 offset:26624\n\tv_fma_f32 v179, v179, v179, 0.5\n\tv_fma_f32 v177, -v145, v177, v145\n\tv_mfma_f32_32x32x16_bf16 v[92:107], v[52:55], a[208:211], v[92:107]\n\tds_read_b64_tr_b16 v[80:81], v11 offset:9216\n\tv_exp_f32 v181, v149\n\tds_read_b64_tr_b16 v[82:83], v11 offset:27648\n\tv_fma_f32 v180, v180, s69, s69\n\tglobal_load_lds_dwordx4 v7, s[98:99] offset:2048\n\tv_rcp_f32 v179, v179\n\tv_mfma_f32_32x32x16_bf16 v[108:123], v[52:55], a[212:215], v[108:123]\n\tds_read_b64_tr_b16 v[84:85], v11 offset:10240\n\ts_mov_b32 s95, 160768\n\tv_lshl_add_u32 v198, v0, 4, s95\n\tv_fma_f32 v180, v180, v180, 0.5\n\tv_fma_f32 v178, -v146, v178, v146\n\tv_and_b32 v13, 15, v0\n\ts_and_b32 s94, s62, 1\n\ts_mul_i32 s95, s94, 16\n\tv_add_u32 v13, s95, v13\n\ts_mul_i32 s94, s60, 2\n\ts_lshr_b32 s93, s62, 1\n\ts_add_u32 s95, s94, s93\n\tv_lshl_add_u32 v13, v13, 6, s95\n\tv_mul_u32_u24 v13, 289, v13\n\ts_mul_i32 s95, s61, 36\n\tv_add_u32 v13, s95, v13\n\tv_lshrrev_b32 v222, 4, v0\n\tv_mul_u32_u24 v222, 10, v222\n\tv_add_u32 v13, v13, v222\n\tv_lshlrev_b32 v13, 1, v13\n\tv_add_u32 v220, 128, v197\n\ts_cmp_lt_u32 s62, 2\n\ts_cselect_b64 vcc, -1, 0\n\tv_cndmask_b32 v198, v198, v220, vcc\n\tv_mfma_f32_32x32x16_bf16 v[92:107], v[56:59], a[216:219], v[92:107]\n\tds_read_b64_tr_b16 v[86:87], v11 offset:28672\n\ts_add_u32 m0, s64, 3072\n\tv_cvt_pk_bf16_f32 v190, v176, v177\n\ts_add_u32 s98, s98, 0x6c000\n\ts_addc_u32 s99, s99, 0\n\tds_read_b64_tr_b16 v[88:89], v11 offset:11264\n\ts_mul_i32 s95, s62, 64\n\tv_add_u32 v220, s95, v0\n\tv_add_u32 v220, 0, v220\n\tv_cmp_gt_u32 vcc, 324, v220\n\ts_lshr_b32 s94, s97, 4\n\ts_mul_i32 s95, s94, 324\n\tv_add_u32 v220, s95, v220\n\tv_lshlrev_b32 v220, 7, v220\n\tv_mov_b32 v221, 0x7fffff00\n\tv_cndmask_b32 v220, v221, v220, vcc\n\tbuffer_load_dword v41, v220, s[48:51], s81 offen \n\ts_mul_i32 s95, s62, 64\n\tv_add_u32 v220, s95, v0\n\tv_add_u32 v220, 256, v220\n\tv_cmp_gt_u32 vcc, 324, v220\n\ts_lshr_b32 s94, s97, 4\n\ts_mul_i32 s95, s94, 324\n\tv_add_u32 v220, s95, v220\n\tv_lshlrev_b32 v220, 7, v220\n\tv_mov_b32 v221, 0x7fffff00\n\tv_cndmask_b32 v220, v221, v220, vcc\n\tbuffer_load_dword v41, v220, s[48:51], s81 offen \n\tv_mfma_f32_32x32x16_bf16 v[108:123], v[56:59], a[220:223], v[108:123]\n\tds_read_b64_tr_b16 v[90:91], v11 offset:29696\n\tv_exp_f32 v182, v150\n\tv_fma_f32 v181, v181, s69, s69\n\tv_rcp_f32 v180, v180\n\ts_mul_i32 s93, s61, 327680\n\ts_mul_i32 s92, s60, 64\n\ts_add_u32 s94, s93, s92\n\ts_lshr_b32 s93, s62, 1\n\ts_mul_i32 s92, s93, 2048\n\ts_add_u32 s95, s94, s92\n\ts_add_u32 s79, s95, 18874368\n\tv_mfma_f32_32x32x16_bf16 v[92:107], v[60:63], a[224:227], v[92:107]\n\tbuffer_load_dwordx4 v[14:17], v13, s[28:31], 0 offen\n\tv_fma_f32 v181, v181, v181, 0.5\n\tv_fma_f32 v179, -v147, v179, v147\n\tglobal_load_lds_dwordx4 v5, s[98:99] offset:0\n\tbuffer_load_dword v30, v13, s[28:31], 0 offen offset:16\n\tbuffer_load_dwordx4 v[200:203], v196, s[56:59], s79 offen sc1\n\ts_add_u32 s79, s79, 4096\n\tv_mfma_f32_32x32x16_bf16 v[108:123], v[60:63], a[228:231], v[108:123]\n\tbuffer_load_ushort v25, v13, s[28:31], 0 offen offset:12\n\tv_exp_f32 v183, v151\n\tglobal_load_lds_dwordx4 v6, s[98:99] offset:1024\n\tv_fma_f32 v182, v182, s69, s69\n\tv_rcp_f32 v181, v181\n\tv_mfma_f32_32x32x16_bf16 v[92:107], v[64:67], a[232:235], v[92:107]\n\tbuffer_load_dwordx4 v[26:29], v13, s[36:39], 0 offen\n\tv_fma_f32 v182, v182, v182, 0.5\n\tv_fma_f32 v180, -v148, v180, v148\n\tbuffer_load_dwordx4 v[204:207], v196, s[56:59], s79 offen sc1\n\ts_add_u32 s79, s79, 4096\n\tbuffer_load_dword v31, v13, s[36:39], 0 offen offset:16\n\tglobal_load_lds_dwordx4 v7, s[98:99] offset:2048\n\tv_mfma_f32_32x32x16_bf16 v[108:123], v[64:67], a[236:239], v[108:123]\n\tv_cvt_pk_bf16_f32 v191, v178, v179\n\tbuffer_load_ushort v32, v13, s[36:39], 0 offen offset:12\n\tbuffer_load_dwordx4 v[208:211], v196, s[56:59], s79 offen sc1\n\ts_add_u32 s79, s79, 4096\n\tv_exp_f32 v184, v152\n\tv_fma_f32 v183, v183, s69, s69\n\tds_write_b128 v21, v[188:191] offset:0\n\ts_waitcnt vmcnt(17) lgkmcnt(1)\n\ts_barrier\n\tv_mfma_f32_32x32x16_bf16 v[92:107], v[68:71], a[240:243], v[92:107]\n\tds_read_b64_tr_b16 v[44:45], v11 offset:12288\n\ts_add_u32 m0, s64, 6144\n\tv_rcp_f32 v182, v182\n\ts_add_u32 s98, s98, 0x6c000\n\ts_addc_u32 s99, s99, 0\n\tds_read_b64_tr_b16 v[46:47], v11 offset:30720\n\tv_mfma_f32_32x32x16_bf16 v[108:123], v[68:71], a[244:247], v[108:123]\n\tv_fma_f32 v183, v183, v183, 0.5\n\tv_fma_f32 v181, -v149, v181, v149\n\tds_read_b64_tr_b16 v[48:49], v11 offset:13312\n\tv_exp_f32 v185, v153\n\tv_fma_f32 v184, v184, s69, s69\n\tglobal_load_lds_dwordx4 v5, s[98:99] offset:0\n\tv_mfma_f32_32x32x16_bf16 v[92:107], v[72:75], a[248:251], v[92:107]\n\tds_read_b64_tr_b16 v[50:51], v11 offset:31744\n\tds_read_b128 v[156:159], v33 offset:1024\n\tv_rcp_f32 v183, v183\n\tds_read_b64_tr_b16 v[52:53], v11 offset:14336\n\tv_fma_f32 v184, v184, v184, 0.5\n\tv_mfma_f32_32x32x16_bf16 v[108:123], v[72:75], a[252:255], v[108:123]\n\tglobal_load_lds_dwordx4 v6, s[98:99] offset:1024\n\tds_read_b64_tr_b16 v[54:55], v11 offset:32768\n\tv_fma_f32 v182, -v150, v182, v150\n\tv_mfma_f32_32x32x16_bf16 v[92:107], v[76:79], v[224:227], v[92:107]\n\tv_cvt_pk_bf16_f32 v192, v180, v181\n\tds_read_b64_tr_b16 v[56:57], v11 offset:15360\n\tv_exp_f32 v186, v154\n\tds_read_b128 v[160:163], v33 offset:1056\n\tv_fma_f32 v185, v185, s69, s69\n\tv_mfma_f32_32x32x16_bf16 v[108:123], v[76:79], v[228:231], v[108:123]\n\tds_read_b64_tr_b16 v[58:59], v11 offset:33792\n\tglobal_load_lds_dwordx4 v7, s[98:99] offset:2048\n\tds_read_b64_tr_b16 v[60:61], v11 offset:16384\n\tv_rcp_f32 v184, v184\n\tv_mfma_f32_32x32x16_bf16 v[92:107], v[80:83], v[232:235], v[92:107]\n\tv_fma_f32 v185, v185, v185, 0.5\n\tds_read_b64_tr_b16 v[62:63], v11 offset:34816\n\tv_fma_f32 v183, -v151, v183, v151\n\tv_exp_f32 v187, v155\n\ts_add_u32 m0, s64, 9216\n\tv_mfma_f32_32x32x16_bf16 v[108:123], v[80:83], v[236:239], v[108:123]\n\tds_read_b64_tr_b16 v[64:65], v11 offset:17408\n\tds_read_b64_tr_b16 v[66:67], v11 offset:35840\n\ts_add_u32 s98, s98, 0x6c000\n\ts_addc_u32 s99, s99, 0\n\tv_fma_f32 v186, v186, s69, s69\n\tds_read_b128 v[164:167], v33 offset:1088\n\tv_rcp_f32 v185, v185\n\tv_mfma_f32_32x32x16_bf16 v[92:107], v[84:87], v[240:243], v[92:107]\n\tglobal_load_lds_dwordx4 v5, s[98:99] offset:0\n\tv_fma_f32 v186, v186, v186, 0.5\n\tv_fma_f32 v184, -v152, v184, v152\n\tv_mfma_f32_32x32x16_bf16 v[108:123], v[84:87], v[244:247], v[108:123]\n\tv_cvt_pk_bf16_f32 v193, v182, v183\n\tv_fma_f32 v187, v187, s69, s69\n\tglobal_load_lds_dwordx4 v6, s[98:99] offset:1024\n\tv_rcp_f32 v186, v186\n\tv_mfma_f32_32x32x16_bf16 v[92:107], v[88:91], v[248:251], v[92:107]\n\tds_read_b128 v[168:171], v33 offset:1120\n\tv_fma_f32 v187, v187, v187, 0.5\n\tv_fma_f32 v185, -v153, v185, v153\n\tv_rcp_f32 v187, v187\n\tv_fma_f32 v186, -v154, v186, v154\n\tv_mfma_f32_32x32x16_bf16 v[108:123], v[88:91], v[252:255], v[108:123]\n\tglobal_load_lds_dwordx4 v7, s[98:99] offset:2048\n\tv_cvt_pk_bf16_f32 v194, v184, v185\n\tv_fma_f32 v187, -v155, v187, v155\n\tv_cvt_pk_bf16_f32 v195, v186, v187\n\tds_write_b128 v22, v[192:195] offset:0\n\ts_waitcnt vmcnt(8) lgkmcnt(3)\n\ts_barrier\n\ts_waitcnt lgkmcnt(1)\n\tv_mfma_f32_32x32x16_bf16 v[124:139], v[44:47], a[0:3], v[156:171]\n\tds_read_b64_tr_b16 v[68:69], v11 offset:0\n\ts_add_u32 m0, s64, 12288\n\ts_add_u32 s98, s98, 0x6c000\n\ts_addc_u32 s99, s99, 0\n\tds_read_b64_tr_b16 v[70:71], v11 offset:18432\n\tv_mfma_f32_32x32x16_bf16 v[140:155], v[44:47], a[4:7], v[156:171]\n\tv_exp_f32 v172, v92\n\tds_read_b64_tr_b16 v[72:73], v11 offset:1024\n\tv_exp_f32 v173, v93\n\tglobal_load_lds_dwordx4 v5, s[98:99] offset:0\n\tv_mfma_f32_32x32x16_bf16 v[124:139], v[48:51], a[8:11], v[124:139]\n\tds_read_b64_tr_b16 v[74:75], v11 offset:19456\n\tv_fma_f32 v172, v172, s69, s69\n\tv_fma_f32 v172, v172, v172, 0.5\n\tv_exp_f32 v174, v94\n\tds_read_b64_tr_b16 v[76:77], v11 offset:2048\n\tglobal_load_lds_dwordx4 v6, s[98:99] offset:1024\n\tv_mfma_f32_32x32x16_bf16 v[140:155], v[48:51], a[12:15], v[140:155]\n\tv_fma_f32 v173, v173, s69, s69\n\tds_read_b64_tr_b16 v[78:79], v11 offset:20480\n\tv_rcp_f32 v172, v172\n\tv_fma_f32 v173, v173, v173, 0.5\n\tv_mfma_f32_32x32x16_bf16 v[124:139], v[52:55], a[16:19], v[124:139]\n\tds_read_b64_tr_b16 v[80:81], v11 offset:3072\n\tv_exp_f32 v175, v95\n\tds_read_b64_tr_b16 v[82:83], v11 offset:21504\n\tv_fma_f32 v174, v174, s69, s69\n\tglobal_load_lds_dwordx4 v7, s[98:99] offset:2048\n\tv_mfma_f32_32x32x16_bf16 v[140:155], v[52:55], a[20:23], v[140:155]\n\tv_rcp_f32 v173, v173\n\tds_read_b64_tr_b16 v[84:85], v11 offset:4096\n\tv_fma_f32 v174, v174, v174, 0.5\n\tv_fma_f32 v172, -v92, v172, v92\n\tv_mfma_f32_32x32x16_bf16 v[124:139], v[56:59], a[24:27], v[124:139]\n\tds_read_b64_tr_b16 v[86:87], v11 offset:22528\n\tv_exp_f32 v176, v96\n\ts_add_u32 m0, s64, 15360\n\tv_fma_f32 v175, v175, s69, s69\n\tds_read_b64_tr_b16 v[88:89], v11 offset:5120\n\tv_mfma_f32_32x32x16_bf16 v[140:155], v[56:59], a[28:31], v[140:155]\n\tds_read_b64_tr_b16 v[90:91], v11 offset:23552\n\ts_add_u32 s98, s98, 0x6c000\n\ts_addc_u32 s99, s99, 0\n\tv_rcp_f32 v174, v174\n\tv_fma_f32 v175, v175, v175, 0.5\n\tglobal_load_lds_dwordx4 v5, s[98:99] offset:0\n\tv_mfma_f32_32x32x16_bf16 v[124:139], v[60:63], a[32:35], v[124:139]\n\tv_fma_f32 v173, -v93, v173, v93\n\tv_exp_f32 v177, v97\n\tv_fma_f32 v176, v176, s69, s69\n\tv_rcp_f32 v175, v175\n\tv_mfma_f32_32x32x16_bf16 v[140:155], v[60:63], a[36:39], v[140:155]\n\tglobal_load_lds_dwordx4 v6, s[98:99] offset:1024\n\tv_fma_f32 v176, v176, v176, 0.5\n\tv_fma_f32 v174, -v94, v174, v94\n\tv_mfma_f32_32x32x16_bf16 v[124:139], v[64:67], a[40:43], v[124:139]\n\tv_cvt_pk_bf16_f32 v188, v172, v173\n\tv_exp_f32 v178, v98\n\tv_fma_f32 v177, v177, s69, s69\n\tv_rcp_f32 v176, v176\n\tv_mfma_f32_32x32x16_bf16 v[140:155], v[64:67], a[44:47], v[140:155]\n\tglobal_load_lds_dwordx4 v7, s[98:99] offset:2048\n\tv_fma_f32 v177, v177, v177, 0.5\n\tv_fma_f32 v175, -v95, v175, v95\n\tv_exp_f32 v179, v99\n\ts_waitcnt vmcnt(6) lgkmcnt(0)\n\ts_barrier\n\tv_mfma_f32_32x32x16_bf16 v[124:139], v[68:71], a[48:51], v[124:139]\n\tds_read_b64_tr_b16 v[44:45], v11 offset:6144\n\ts_add_u32 m0, s64, 0\n\tv_fma_f32 v178, v178, s69, s69\n\tv_rcp_f32 v177, v177\n\tds_read_b64_tr_b16 v[46:47], v11 offset:24576\n\ts_add_u32 s98, s98, 0x6c000\n\ts_addc_u32 s99, s99, 0\n\tv_fma_f32 v178, v178, v178, 0.5\n\tv_mfma_f32_32x32x16_bf16 v[140:155], v[68:71], a[52:55], v[140:155]\n\tds_read_b64_tr_b16 v[48:49], v11 offset:7168\n\tglobal_load_lds_dwordx4 v5, s[98:99] offset:0\n\tv_fma_f32 v176, -v96, v176, v96\n\tv_cvt_pk_bf16_f32 v189, v174, v175\n\tv_exp_f32 v180, v100\n\tv_mfma_f32_32x32x16_bf16 v[124:139], v[72:75], a[56:59], v[124:139]\n\tds_read_b64_tr_b16 v[50:51], v11 offset:25600\n\tv_fma_f32 v179, v179, s69, s69\n\tds_read_b64_tr_b16 v[52:53], v11 offset:8192\n\tv_rcp_f32 v178, v178\n\tds_write_b128 v197, v[200:203] offset:0\n\tv_mfma_f32_32x32x16_bf16 v[140:155], v[72:75], a[60:63], v[140:155]\n\tglobal_load_lds_dwordx4 v6, s[98:99] offset:1024\n\tds_read_b64_tr_b16 v[54:55], v11 offset:26624\n\tv_fma_f32 v179, v179, v179, 0.5\n\tv_mfma_f32_32x32x16_bf16 v[124:139], v[76:79], a[64:67], v[124:139]\n\tds_read_b64_tr_b16 v[56:57], v11 offset:9216\n\tv_fma_f32 v177, -v97, v177, v97\n\tv_exp_f32 v181, v101\n\tds_read_b64_tr_b16 v[58:59], v11 offset:27648\n\tv_fma_f32 v180, v180, s69, s69\n\tglobal_load_lds_dwordx4 v7, s[98:99] offset:2048\n\tv_mfma_f32_32x32x16_bf16 v[140:155], v[76:79], a[68:71], v[140:155]\n\tv_rcp_f32 v179, v179\n\tds_read_b64_tr_b16 v[60:61], v11 offset:10240\n\tv_fma_f32 v180, v180, v180, 0.5\n\tv_fma_f32 v178, -v98, v178, v98\n\tds_write_b128 v197, v[204:207] offset:64\n\tv_mfma_f32_32x32x16_bf16 v[124:139], v[80:83], a[72:75], v[124:139]\n\tds_read_b64_tr_b16 v[62:63], v11 offset:28672\n\ts_add_u32 m0, s64, 3072\n\tv_cvt_pk_bf16_f32 v190, v176, v177\n\ts_add_u32 s98, s98, 0x6c000\n\ts_addc_u32 s99, s99, 0\n\tds_read_b64_tr_b16 v[64:65], v11 offset:11264\n\tv_mfma_f32_32x32x16_bf16 v[140:155], v[80:83], a[76:79], v[140:155]\n\tds_read_b64_tr_b16 v[66:67], v11 offset:29696\n\tv_exp_f32 v182, v102\n\tv_fma_f32 v181, v181, s69, s69\n\tv_rcp_f32 v180, v180\n\tv_mfma_f32_32x32x16_bf16 v[124:139], v[84:87], a[80:83], v[124:139]\n\tglobal_load_lds_dwordx4 v5, s[98:99] offset:0\n\tv_fma_f32 v181, v181, v181, 0.5\n\tv_fma_f32 v179, -v99, v179, v99\n\tds_write_b128 v198, v[208:211]\n\tv_mfma_f32_32x32x16_bf16 v[140:155], v[84:87], a[84:87], v[140:155]\n\tv_exp_f32 v183, v103\n\tglobal_load_lds_dwordx4 v6, s[98:99] offset:1024\n\tv_mfma_f32_32x32x16_bf16 v[124:139], v[88:91], a[88:91], v[124:139]\n\tv_fma_f32 v182, v182, s69, s69\n\tv_rcp_f32 v181, v181\n\tv_fma_f32 v182, v182, v182, 0.5\n\tv_fma_f32 v180, -v100, v180, v100\n\tv_cvt_pk_bf16_f32 v191, v178, v179\n\tv_mfma_f32_32x32x16_bf16 v[140:155], v[88:91], a[92:95], v[140:155]\n\tglobal_load_lds_dwordx4 v7, s[98:99] offset:2048\n\tv_exp_f32 v184, v104\n\tv_fma_f32 v183, v183, s69, s69\n\tds_write_b128 v19, v[188:191] offset:2048\n\ts_waitcnt vmcnt(6) lgkmcnt(0)\n\ts_barrier\n\tv_mfma_f32_32x32x16_bf16 v[124:139], v[44:47], a[96:99], v[124:139]\n\tds_read_b64_tr_b16 v[68:69], v11 offset:12288\n\ts_add_u32 m0, s64, 6144\n\tv_rcp_f32 v182, v182\n\ts_add_u32 s98, s98, 0x6c000\n\ts_addc_u32 s99, s99, 0\n\tv_bfe_u32 v13, v0, 2, 2\n\tv_lshrrev_b32 v222, 4, v0\n\tv_lshl_add_u32 v13, v222, 3, v13\n\tv_mul_u32_u24 v13, 336, v13\n\tv_and_b32 v222, 3, v0\n\tv_lshl_add_u32 v13, v222, 3, v13\n\ts_lshr_b32 s94, s62, 1\n\ts_mul_i32 s95, s94, 160\n\tv_add_u32 v13, s95, v13\n\tv_add_u32 v13, 150016, v13\n\tv_mfma_f32_32x32x16_bf16 v[140:155], v[44:47], a[100:103], v[140:155]\n\tds_read_b64_tr_b16 v[70:71], v11 offset:30720\n\tv_fma_f32 v183, v183, v183, 0.5\n\tv_fma_f32 v181, -v101, v181, v101\n\tds_read_b64_tr_b16 v[72:73], v11 offset:13312\n\tv_exp_f32 v185, v105\n\tglobal_load_lds_dwordx4 v5, s[98:99] offset:0\n\tds_read_b64_tr_b16 v[196:197], v13 offset:0\n\tv_mfma_f32_32x32x16_bf16 v[124:139], v[48:51], a[104:107], v[124:139]\n\tds_read_b64_tr_b16 v[74:75], v11 offset:31744\n\tds_read_b64_tr_b16 v[198:199], v13 offset:1344\n\tv_fma_f32 v184, v184, s69, s69\n\tds_read_b64_tr_b16 v[200:201], v13 offset:32\n\tds_read_b64_tr_b16 v[202:203], v13 offset:1376\n\tds_read_b64_tr_b16 v[76:77], v11 offset:14336\n\tv_rcp_f32 v183, v183\n\ts_waitcnt lgkmcnt(3)\n\tv_mfma_f32_16x16x32_bf16 v[204:207], v[196:199], v[212:215], 0\n\tv_fma_f32 v184, v184, v184, 0.5\n\tv_mfma_f32_16x16x32_bf16 v[208:211], v[196:199], v[216:219], 0\n\tv_mfma_f32_32x32x16_bf16 v[140:155], v[48:51], a[108:111], v[140:155]\n\tglobal_load_lds_dwordx4 v6, s[98:99] offset:1024\n\tv_fma_f32 v182, -v102, v182, v102\n\tds_read_b64_tr_b16 v[78:79], v11 offset:32768\n\tv_cvt_pk_bf16_f32 v192, v180, v181\n\tds_read_b64_tr_b16 v[196:197], v13 offset:64\n\tds_read_b64_tr_b16 v[198:199], v13 offset:1408\n\tv_exp_f32 v186, v106\n\tv_mfma_f32_32x32x16_bf16 v[124:139], v[52:55], a[112:115], v[124:139]\n\tds_read_b64_tr_b16 v[80:81], v11 offset:15360\n\tv_sub_f32 v204, v204, v209\n\tv_add_f32 v205, v205, v208\n\tv_sub_f32 v206, v206, v211\n\tv_add_f32 v207, v207, v210\n\tv_fma_f32 v185, v185, s69, s69\n\tv_lshlrev_b32 v220, 16, v14\n\tv_lshlrev_b32 v221, 16, v26\n\tv_rcp_f32 v184, v184\n\tv_and_b32 v222, s77, v14\n\tglobal_load_lds_dwordx4 v7, s[98:99] offset:2048\n\tv_mfma_f32_32x32x16_bf16 v[140:155], v[52:55], a[116:119], v[140:155]\n\tds_read_b64_tr_b16 v[82:83], v11 offset:33792\n\tv_and_b32 v223, s77, v26\n\tv_mul_f32 v36, v204, v220\n\tv_mul_f32 v37, v204, v221\n\tv_fma_f32 v185, v185, v185, 0.5\n\tv_fma_f32 v36, -v205, v221, v36\n\tds_read_b64_tr_b16 v[84:85], v11 offset:16384\n\tv_fma_f32 v37, v205, v220, v37\n\tv_fma_f32 v183, -v103, v183, v103\n\tv_cvt_pk_bf16_f32 v14, v36, v37\n\tv_mul_f32 v36, v206, v222\n\tv_exp_f32 v187, v107\n\tv_mfma_f32_32x32x16_bf16 v[124:139], v[56:59], a[120:123], v[124:139]\n\tds_read_b64_tr_b16 v[86:87], v11 offset:34816\n\tv_mul_f32 v37, v206, v223\n\tv_fma_f32 v36, -v207, v223, v36\n\tv_fma_f32 v37, v207, v222, v37\n\tv_cvt_pk_bf16_f32 v26, v36, v37\n\ts_add_u32 m0, s64, 9216\n\tv_fma_f32 v186, v186, s69, s69\n\ts_waitcnt lgkmcnt(8)\n\tv_mfma_f32_16x16x32_bf16 v[204:207], v[200:203], v[212:215], 0\n\ts_add_u32 s98, s98, 0x6c000\n\ts_addc_u32 s99, s99, 0\n\tv_mfma_f32_32x32x16_bf16 v[140:155], v[56:59], a[124:127], v[140:155]\n\tds_read_b64_tr_b16 v[88:89], v11 offset:17408\n\tds_read_b64_tr_b16 v[90:91], v11 offset:35840\n\tv_rcp_f32 v185, v185\n\tv_fma_f32 v186, v186, v186, 0.5\n\tv_fma_f32 v184, -v104, v184, v104\n\tv_mfma_f32_16x16x32_bf16 v[208:211], v[200:203], v[216:219], 0\n\tglobal_load_lds_dwordx4 v5, s[98:99] offset:0\n\tv_mfma_f32_32x32x16_bf16 v[124:139], v[60:63], a[128:131], v[124:139]\n\tds_read_b64_tr_b16 v[200:201], v13 offset:96\n\tds_read_b64_tr_b16 v[202:203], v13 offset:1440\n\tv_cvt_pk_bf16_f32 v193, v182, v183\n\ts_nop 2\n\tv_sub_f32 v204, v204, v209\n\tv_add_f32 v205, v205, v208\n\tv_sub_f32 v206, v206, v211\n\tv_exp_f32 v172, v108\n\tv_add_f32 v207, v207, v210\n\tv_lshlrev_b32 v220, 16, v15\n\tv_lshlrev_b32 v221, 16, v27\n\tv_fma_f32 v187, v187, s69, s69\n\tv_and_b32 v222, s77, v15\n\tv_mfma_f32_32x32x16_bf16 v[140:155], v[60:63], a[132:135], v[140:155]\n\tv_and_b32 v223, s77, v27\n\tv_rcp_f32 v186, v186\n\tv_mul_f32 v36, v204, v220\n\tv_mul_f32 v37, v204, v221\n\tv_fma_f32 v36, -v205, v221, v36\n\tglobal_load_lds_dwordx4 v6, s[98:99] offset:1024\n\tv_fma_f32 v37, v205, v220, v37\n\tv_fma_f32 v187, v187, v187, 0.5\n\tv_cvt_pk_bf16_f32 v15, v36, v37\n\tv_mul_f32 v36, v206, v222\n\tv_mfma_f32_32x32x16_bf16 v[124:139], v[64:67], a[136:139], v[124:139]\n\tv_fma_f32 v185, -v105, v185, v105\n\tv_mul_f32 v37, v206, v223\n\tv_fma_f32 v36, -v207, v223, v36\n\tv_exp_f32 v173, v109\n\tv_fma_f32 v37, v207, v222, v37\n\tv_cvt_pk_bf16_f32 v27, v36, v37\n\ts_waitcnt lgkmcnt(8)\n\tv_mfma_f32_16x16x32_bf16 v[204:207], v[196:199], v[212:215], 0\n\tglobal_load_lds_dwordx4 v7, s[98:99] offset:2048\n\tv_mfma_f32_32x32x16_bf16 v[140:155], v[64:67], a[140:143], v[140:155]\n\tv_fma_f32 v172, v172, s69, s69\n\tv_rcp_f32 v187, v187\n\tv_mfma_f32_16x16x32_bf16 v[208:211], v[196:199], v[216:219], 0\n\tv_fma_f32 v172, v172, v172, 0.5\n\tds_read_b64_tr_b16 v[196:197], v13 offset:128\n\tds_read_b64_tr_b16 v[198:199], v13 offset:1472\n\tv_fma_f32 v186, -v106, v186, v106\n\ts_nop 3\n\tv_sub_f32 v204, v204, v209\n\tv_add_f32 v205, v205, v208\n\ts_waitcnt vmcnt(6) lgkmcnt(4)\n\ts_barrier\n\tv_mfma_f32_32x32x16_bf16 v[124:139], v[68:71], a[144:147], v[124:139]\n\tds_read_b64_tr_b16 v[44:45], v11 offset:0\n\tv_cvt_pk_bf16_f32 v194, v184, v185\n\tv_sub_f32 v206, v206, v211\n\ts_add_u32 m0, s64, 12288\n\tv_add_f32 v207, v207, v210\n\tv_lshlrev_b32 v220, 16, v16\n\tv_exp_f32 v174, v110\n\ts_sub_u32 s98, s98, 0x4a3f00\n\ts_subb_u32 s99, s99, 0\n\tds_read_b64_tr_b16 v[46:47], v11 offset:18432\n\tv_lshlrev_b32 v221, 16, v28\n\tv_mfma_f32_32x32x16_bf16 v[140:155], v[68:71], a[148:151], v[140:155]\n\tv_and_b32 v222, s77, v16\n\tv_and_b32 v223, s77, v28\n\tv_fma_f32 v173, v173, s69, s69\n\tds_read_b64_tr_b16 v[48:49], v11 offset:1024\n\tv_mul_f32 v36, v204, v220\n\tv_mul_f32 v37, v204, v221\n\tv_rcp_f32 v172, v172\n\tglobal_load_lds_dwordx4 v5, s[98:99] offset:0\n\tv_fma_f32 v36, -v205, v221, v36\n\tv_mfma_f32_32x32x16_bf16 v[124:139], v[72:75], a[152:155], v[124:139]\n\tds_read_b64_tr_b16 v[50:51], v11 offset:19456\n\tv_fma_f32 v37, v205, v220, v37\n\tv_cvt_pk_bf16_f32 v16, v36, v37\n\tv_fma_f32 v173, v173, v173, 0.5\n\tv_mul_f32 v36, v206, v222\n\tv_fma_f32 v187, -v107, v187, v107\n\tv_mul_f32 v37, v206, v223\n\tv_fma_f32 v36, -v207, v223, v36\n\tv_exp_f32 v175, v111\n\tv_fma_f32 v37, v207, v222, v37\n\tds_read_b64_tr_b16 v[52:53], v11 offset:2048\n\tv_mfma_f32_32x32x16_bf16 v[140:155], v[72:75], a[156:159], v[140:155]\n\tv_cvt_pk_bf16_f32 v28, v36, v37\n\tglobal_load_lds_dwordx4 v6, s[98:99] offset:1024\n\tv_fma_f32 v174, v174, s69, s69\n\ts_waitcnt lgkmcnt(7)\n\tv_mfma_f32_16x16x32_bf16 v[204:207], v[200:203], v[212:215], 0\n\tds_read_b64_tr_b16 v[54:55], v11 offset:20480\n\tv_rcp_f32 v173, v173\n\tv_mfma_f32_32x32x16_bf16 v[124:139], v[76:79], a[160:163], v[124:139]\n\tds_read_b64_tr_b16 v[56:57], v11 offset:3072\n\tv_mfma_f32_16x16x32_bf16 v[208:211], v[200:203], v[216:219], 0\n\tv_fma_f32 v174, v174, v174, 0.5\n\tv_fma_f32 v172, -v108, v172, v108\n\ts_nop 5\n\tv_sub_f32 v204, v204, v209\n\tglobal_load_lds_dwordx4 v7, s[98:99] offset:2048\n\tv_mfma_f32_32x32x16_bf16 v[140:155], v[76:79], a[164:167], v[140:155]\n\tds_read_b64_tr_b16 v[58:59], v11 offset:21504\n\tv_add_f32 v205, v205, v208\n\tv_cvt_pk_bf16_f32 v195, v186, v187\n\tv_sub_f32 v206, v206, v211\n\tv_add_f32 v207, v207, v210\n\tv_lshlrev_b32 v220, 16, v25\n\tv_exp_f32 v176, v112\n\tds_read_b64_tr_b16 v[60:61], v11 offset:4096\n\tv_lshlrev_b32 v221, 16, v32\n\tv_and_b32 v222, s77, v17\n\tv_fma_f32 v175, v175, s69, s69\n\tv_and_b32 v223, s77, v29\n\tv_mfma_f32_32x32x16_bf16 v[124:139], v[80:83], a[168:171], v[124:139]\n\tds_read_b64_tr_b16 v[62:63], v11 offset:22528\n\tv_mul_f32 v36, v204, v220\n\tv_rcp_f32 v174, v174\n\tv_mul_f32 v37, v204, v221\n\ts_add_u32 m0, s64, 15360\n\tv_fma_f32 v36, -v205, v221, v36\n\tv_fma_f32 v37, v205, v220, v37\n\tv_fma_f32 v175, v175, v175, 0.5\n\tv_cvt_pk_bf16_f32 v17, v36, v37\n\ts_add_u32 s98, s98, 0x6c000\n\ts_addc_u32 s99, s99, 0\n\tv_mfma_f32_32x32x16_bf16 v[140:155], v[80:83], a[172:175], v[140:155]\n\tds_read_b64_tr_b16 v[64:65], v11 offset:5120\n\tds_read_b64_tr_b16 v[66:67], v11 offset:23552\n\tv_mul_f32 v36, v206, v222\n\tv_fma_f32 v173, -v109, v173, v109\n\tv_mul_f32 v37, v206, v223\n\tv_fma_f32 v36, -v207, v223, v36\n\tv_exp_f32 v177, v113\n\tv_fma_f32 v37, v207, v222, v37\n\tglobal_load_lds_dwordx4 v5, s[98:99] offset:0\n\tv_mfma_f32_32x32x16_bf16 v[124:139], v[84:87], a[176:179], v[124:139]\n\tv_cvt_pk_bf16_f32 v29, v36, v37\n\ts_waitcnt lgkmcnt(12)\n\tv_mfma_f32_16x16x32_bf16 v[204:207], v[196:199], v[212:215], 0\n\tv_fma_f32 v176, v176, s69, s69\n\tv_rcp_f32 v175, v175\n\tv_mfma_f32_16x16x32_bf16 v[208:211], v[196:199], v[216:219], 0\n\tv_mfma_f32_32x32x16_bf16 v[140:155], v[84:87], a[180:183], v[140:155]\n\tv_fma_f32 v176, v176, v176, 0.5\n\tv_fma_f32 v174, -v110, v174, v110\n\tglobal_load_lds_dwordx4 v6, s[98:99] offset:1024\n\ts_nop 3\n\tv_sub_f32 v204, v204, v209\n\tv_cvt_pk_bf16_f32 v188, v172, v173\n\tv_add_f32 v205, v205, v208\n\tv_sub_f32 v206, v206, v211\n\tv_exp_f32 v178, v114\n\tv_add_f32 v207, v207, v210\n\tv_mfma_f32_32x32x16_bf16 v[124:139], v[88:91], a[184:187], v[124:139]\n\tv_lshlrev_b32 v220, 16, v30\n\tv_lshlrev_b32 v221, 16, v31\n\tv_and_b32 v222, s77, v30\n\tv_fma_f32 v177, v177, s69, s69\n\tv_and_b32 v223, s77, v31\n\tv_mul_f32 v36, v204, v220\n\tv_rcp_f32 v176, v176\n\tglobal_load_lds_dwordx4 v7, s[98:99] offset:2048\n\tv_mfma_f32_32x32x16_bf16 v[140:155], v[88:91], a[188:191], v[140:155]\n\tv_mul_f32 v37, v204, v221\n\tv_fma_f32 v36, -v205, v221, v36\n\tv_fma_f32 v37, v205, v220, v37\n\tv_fma_f32 v177, v177, v177, 0.5\n\tv_cvt_pk_bf16_f32 v30, v36, v37\n\tv_fma_f32 v175, -v111, v175, v111\n\tv_mul_f32 v36, v206, v222\n\tv_mul_f32 v37, v206, v223\n\tv_exp_f32 v179, v115\n\tv_fma_f32 v36, -v207, v223, v36\n\tv_fma_f32 v37, v207, v222, v37\n\tv_cvt_pk_bf16_f32 v31, v36, v37\n\tds_write_b128 v20, v[192:195] offset:2048\n\ts_waitcnt vmcnt(6) lgkmcnt(1)\n\ts_barrier\n\tv_mfma_f32_32x32x16_bf16 v[124:139], v[44:47], a[192:195], v[124:139]\n\tds_read_b64_tr_b16 v[68:69], v11 offset:6144\n\ts_add_u32 m0, s64, 0\n\tv_fma_f32 v178, v178, s69, s69\n\tv_rcp_f32 v177, v177\n\tds_read_b64_tr_b16 v[70:71], v11 offset:24576\n\ts_add_u32 s98, s98, 0x6c000\n\ts_addc_u32 s99, s99, 0\n\tv_fma_f32 v178, v178, v178, 0.5\n\tv_mfma_f32_32x32x16_bf16 v[140:155], v[44:47], a[196:199], v[140:155]\n\tds_read_b64_tr_b16 v[72:73], v11 offset:7168\n\tglobal_load_lds_dwordx4 v5, s[98:99] offset:0\n\tv_fma_f32 v176, -v112, v176, v112\n\tv_and_b32 v13, 15, v0\n\ts_lshr_b32 s93, s62, 1\n\ts_mul_i32 s94, s93, 32\n\ts_and_b32 s92, s62, 1\n\ts_mul_i32 s93, s92, 16\n\ts_add_u32 s95, s94, s93\n\tv_add_u32 v13, s95, v13\n\tv_mul_u32_u24 v13, 168, v13\n\tv_lshrrev_b32 v222, 4, v0\n\tv_lshl_add_u32 v13, v222, 3, v13\n\tv_add_u32 v13, 150016, v13\n\tv_mfma_f32_32x32x16_bf16 v[124:139], v[48:51], a[200:203], v[124:139]\n\tds_read_b64_tr_b16 v[74:75], v11 offset:25600\n\tv_cvt_pk_bf16_f32 v189, v174, v175\n\tv_exp_f32 v180, v116\n\tv_fma_f32 v179, v179, s69, s69\n\tds_read_b64_tr_b16 v[76:77], v11 offset:8192\n\tv_mfma_f32_32x32x16_bf16 v[140:155], v[48:51], a[204:207], v[140:155]\n\tv_rcp_f32 v178, v178\n\tglobal_load_lds_dwordx4 v6, s[98:99] offset:1024\n\tds_read_b64_tr_b16 v[78:79], v11 offset:26624\n\tv_fma_f32 v179, v179, v179, 0.5\n\tv_mfma_f32_32x32x16_bf16 v[124:139], v[52:55], a[208:211], v[124:139]\n\tds_read_b64_tr_b16 v[80:81], v11 offset:9216\n\tv_fma_f32 v177, -v113, v177, v113\n\tv_exp_f32 v181, v117\n\tds_read_b64_tr_b16 v[82:83], v11 offset:27648\n\tv_fma_f32 v180, v180, s69, s69\n\tds_write2_b32 v13, v14, v26 offset0:0 offset1:1\n\tv_mfma_f32_32x32x16_bf16 v[140:155], v[52:55], a[212:215], v[140:155]\n\tglobal_load_lds_dwordx4 v7, s[98:99] offset:2048\n\tv_rcp_f32 v179, v179\n\tds_read_b64_tr_b16 v[84:85], v11 offset:10240\n\tds_write2_b32 v13, v15, v27 offset0:8 offset1:9\n\tv_mfma_f32_32x32x16_bf16 v[124:139], v[56:59], a[216:219], v[124:139]\n\tds_read_b64_tr_b16 v[86:87], v11 offset:28672\n\tv_fma_f32 v180, v180, v180, 0.5\n\tv_fma_f32 v178, -v114, v178, v114\n\ts_add_u32 m0, s64, 3072\n\tv_cvt_pk_bf16_f32 v190, v176, v177\n\tds_read_b64_tr_b16 v[88:89], v11 offset:11264\n\tv_mfma_f32_32x32x16_bf16 v[140:155], v[56:59], a[220:223], v[140:155]\n\tds_read_b64_tr_b16 v[90:91], v11 offset:29696\n\ts_add_u32 s98, s98, 0x6c000\n\ts_addc_u32 s99, s99, 0\n\tv_exp_f32 v182, v118\n\tv_fma_f32 v181, v181, s69, s69\n\tds_write2_b32 v13, v16, v28 offset0:16 offset1:17\n\tv_mfma_f32_32x32x16_bf16 v[124:139], v[60:63], a[224:227], v[124:139]\n\tglobal_load_lds_dwordx4 v5, s[98:99] offset:0\n\tv_rcp_f32 v180, v180\n\tv_fma_f32 v181, v181, v181, 0.5\n\tv_fma_f32 v179, -v115, v179, v115\n\tv_mfma_f32_32x32x16_bf16 v[140:155], v[60:63], a[228:231], v[140:155]\n\tv_exp_f32 v183, v119\n\tglobal_load_lds_dwordx4 v6, s[98:99] offset:1024\n\tds_write2_b32 v13, v17, v29 offset0:24 offset1:25\n\tv_mfma_f32_32x32x16_bf16 v[124:139], v[64:67], a[232:235], v[124:139]\n\tv_fma_f32 v182, v182, s69, s69\n\tv_rcp_f32 v181, v181\n\tv_fma_f32 v182, v182, v182, 0.5\n\tv_fma_f32 v180, -v116, v180, v116\n\tv_cvt_pk_bf16_f32 v191, v178, v179\n\tds_write2_b32 v13, v30, v31 offset0:32 offset1:33\n\tv_mfma_f32_32x32x16_bf16 v[140:155], v[64:67], a[236:239], v[140:155]\n\tglobal_load_lds_dwordx4 v7, s[98:99] offset:2048\n\tv_exp_f32 v184, v120\n\tv_fma_f32 v183, v183, s69, s69\n\tds_write_b128 v21, v[188:191] offset:2048\n\ts_waitcnt vmcnt(6) lgkmcnt(0)\n\ts_barrier\n\tv_mfma_f32_32x32x16_bf16 v[124:139], v[68:71], a[240:243], v[124:139]\n\tds_read_b64_tr_b16 v[44:45], v11 offset:12288\n\ts_add_u32 m0, s64, 6144\n\tv_rcp_f32 v182, v182\n\ts_add_u32 s98, s98, 0x6c000\n\ts_addc_u32 s99, s99, 0\n\tv_lshrrev_b32 v13, 4, v0\n\ts_lshr_b32 s94, s62, 1\n\ts_mul_i32 s95, s94, 32\n\tv_lshl_add_u32 v13, v13, 3, s95\n\tv_bfe_u32 v222, v0, 2, 2\n\tv_add_u32 v13, v13, v222\n\tv_mul_u32_u24 v13, 168, v13\n\tv_and_b32 v222, 3, v0\n\tv_lshl_add_u32 v13, v222, 3, v13\n\tv_add_u32 v13, 150016, v13\n\tv_mfma_f32_32x32x16_bf16 v[140:155], v[68:71], a[244:247], v[140:155]\n\tds_read_b64_tr_b16 v[46:47], v11 offset:30720\n\tv_fma_f32 v183, v183, v183, 0.5\n\tv_fma_f32 v181, -v117, v181, v117\n\tds_read_b64_tr_b16 v[48:49], v11 offset:13312\n\tv_exp_f32 v185, v121\n\tglobal_load_lds_dwordx4 v5, s[98:99] offset:0\n\tv_mfma_f32_32x32x16_bf16 v[124:139], v[72:75], a[248:251], v[124:139]\n\tds_read_b64_tr_b16 v[50:51], v11 offset:31744\n\tds_read_b128 v[156:159], v33 offset:1536\n\tv_fma_f32 v184, v184, s69, s69\n\tv_rcp_f32 v183, v183\n\tds_read_b64_tr_b16 v[52:53], v11 offset:14336\n\tv_and_b32 v18, 15, v0\n\ts_and_b32 s94, s62, 1\n\ts_mul_i32 s95, s94, 16\n\tv_add_u32 v18, s95, v18\n\tv_mul_u32_u24 v18, 10240, v18\n\tv_lshrrev_b32 v222, 4, v0\n\tv_lshl_add_u32 v18, v222, 3, v18\n\tv_mfma_f32_32x32x16_bf16 v[140:155], v[72:75], a[252:255], v[140:155]\n\tglobal_load_lds_dwordx4 v6, s[98:99] offset:1024\n\tv_fma_f32 v184, v184, v184, 0.5\n\tds_read_b64_tr_b16 v[54:55], v11 offset:32768\n\ts_mul_i32 s94, s61, 327680\n\ts_mul_i32 s91, s60, 2\n\ts_lshr_b32 s90, s62, 1\n\ts_add_u32 s92, s91, s90\n\ts_mul_i32 s93, s92, 160\n\ts_add_u32 s95, s94, s93\n\ts_add_u32 s79, s95, 21495808\n\tv_mfma_f32_32x32x16_bf16 v[124:139], v[76:79], v[224:227], v[124:139]\n\tv_fma_f32 v182, -v118, v182, v118\n\tv_cvt_pk_bf16_f32 v192, v180, v181\n\tds_read_b64_tr_b16 v[56:57], v11 offset:15360\n\tv_exp_f32 v186, v122\n\tds_read_b64_tr_b16 v[196:197], v13 offset:0\n\tds_read_b64_tr_b16 v[198:199], v13 offset:672\n\tds_read_b64_tr_b16 v[200:201], v13 offset:32\n\tglobal_load_lds_dwordx4 v7, s[98:99] offset:2048\n\tds_read_b128 v[160:163], v33 offset:1568\n\tv_mfma_f32_32x32x16_bf16 v[140:155], v[76:79], v[228:231], v[140:155]\n\tds_read_b64_tr_b16 v[58:59], v11 offset:33792\n\tv_fma_f32 v185, v185, s69, s69\n\tds_read_b64_tr_b16 v[202:203], v13 offset:704\n\tv_rcp_f32 v184, v184\n\tds_read_b64_tr_b16 v[60:61], v11 offset:16384\n\ts_waitcnt lgkmcnt(5)\n\tv_mfma_f32_16x16x32_bf16 v[204:207], v[196:199], v[212:215], 0\n\tv_mfma_f32_16x16x32_bf16 v[208:211], v[196:199], v[216:219], 0\n\tv_mfma_f32_32x32x16_bf16 v[124:139], v[80:83], v[232:235], v[124:139]\n\tv_fma_f32 v185, v185, v185, 0.5\n\tds_read_b64_tr_b16 v[62:63], v11 offset:34816\n\tv_fma_f32 v183, -v119, v183, v119\n\tv_exp_f32 v187, v123\n\ts_add_u32 m0, s64, 9216\n\tds_read_b64_tr_b16 v[196:197], v13 offset:64\n\tds_read_b64_tr_b16 v[198:199], v13 offset:736\n\tv_add_f32 v204, v204, v209\n\ts_add_u32 s98, s98, 0x6c000\n\ts_addc_u32 s99, s99, 0\n\tds_read_b64_tr_b16 v[64:65], v11 offset:17408\n\tv_mfma_f32_32x32x16_bf16 v[140:155], v[80:83], v[236:239], v[140:155]\n\tds_read_b64_tr_b16 v[66:67], v11 offset:35840\n\tv_sub_f32 v205, v205, v208\n\tv_fma_f32 v186, v186, s69, s69\n\tv_add_f32 v206, v206, v211\n\tds_read_b128 v[164:167], v33 offset:1600\n\tv_sub_f32 v207, v207, v210\n\tv_rcp_f32 v185, v185\n\tv_cvt_pk_bf16_f32 v14, v204, v205\n\tv_cvt_pk_bf16_f32 v15, v206, v207\n\tglobal_load_lds_dwordx4 v5, s[98:99] offset:0\n\tv_mfma_f32_32x32x16_bf16 v[124:139], v[84:87], v[240:243], v[124:139]\n\tbuffer_store_dwordx2 v[14:15], v18, s[56:59], s79 offen offset:0 sc1\n\tv_fma_f32 v186, v186, v186, 0.5\n\tv_fma_f32 v184, -v120, v184, v120\n\tv_cvt_pk_bf16_f32 v193, v182, v183\n\ts_waitcnt lgkmcnt(7)\n\tv_mfma_f32_16x16x32_bf16 v[204:207], v[200:203], v[212:215], 0\n\tv_mfma_f32_32x32x16_bf16 v[140:155], v[84:87], v[244:247], v[140:155]\n\tv_fma_f32 v187, v187, s69, s69\n\tglobal_load_lds_dwordx4 v6, s[98:99] offset:1024\n\tds_read_b128 v[168:171], v33 offset:1632\n\tv_rcp_f32 v186, v186\n\tv_mfma_f32_16x16x32_bf16 v[208:211], v[200:203], v[216:219], 0\n\tv_mfma_f32_32x32x16_bf16 v[124:139], v[88:91], v[248:251], v[124:139]\n\tv_fma_f32 v187, v187, v187, 0.5\n\tds_read_b64_tr_b16 v[200:201], v13 offset:96\n\tv_fma_f32 v185, -v121, v185, v121\n\tds_read_b64_tr_b16 v[202:203], v13 offset:768\n\ts_nop 2\n\tv_add_f32 v204, v204, v209\n\tv_sub_f32 v205, v205, v208\n\tv_rcp_f32 v187, v187\n\tv_add_f32 v206, v206, v211\n\tglobal_load_lds_dwordx4 v7, s[98:99] offset:2048\n\tv_mfma_f32_32x32x16_bf16 v[140:155], v[88:91], v[252:255], v[140:155]\n\tv_sub_f32 v207, v207, v210\n\tv_cvt_pk_bf16_f32 v16, v204, v205\n\tv_fma_f32 v186, -v122, v186, v122\n\tv_cvt_pk_bf16_f32 v17, v206, v207\n\tbuffer_store_dwordx2 v[16:17], v18, s[56:59], s79 offen offset:32 sc1\n\tv_cvt_pk_bf16_f32 v194, v184, v185\n\tv_fma_f32 v187, -v123, v187, v123\n\tv_cvt_pk_bf16_f32 v195, v186, v187\n\tds_write_b128 v22, v[192:195] offset:2048\n\ts_waitcnt vmcnt(8) lgkmcnt(5)\n\ts_barrier\n\ts_waitcnt lgkmcnt(3)\n\tv_mfma_f32_32x32x16_bf16 v[92:107], v[44:47], a[0:3], v[156:171]\n\tds_read_b64_tr_b16 v[68:69], v11 offset:0\n\tv_mfma_f32_16x16x32_bf16 v[204:207], v[196:199], v[212:215], 0\n\ts_add_u32 m0, s64, 12288\n\ts_add_u32 s98, s98, 0x6c000\n\ts_addc_u32 s99, s99, 0\n\tds_read_b64_tr_b16 v[70:71], v11 offset:18432\n\tv_mfma_f32_16x16x32_bf16 v[208:211], v[196:199], v[216:219], 0\n\tv_mfma_f32_32x32x16_bf16 v[108:123], v[44:47], a[4:7], v[156:171]\n\tv_exp_f32 v172, v124\n\tds_read_b64_tr_b16 v[72:73], v11 offset:1024\n\tv_exp_f32 v173, v125\n\tds_read_b64_tr_b16 v[196:197], v13 offset:128\n\tglobal_load_lds_dwordx4 v5, s[98:99] offset:0\n\tds_read_b64_tr_b16 v[198:199], v13 offset:800\n\tv_fma_f32 v172, v172, s69, s69\n\tv_mfma_f32_32x32x16_bf16 v[92:107], v[48:51], a[8:11], v[92:107]\n\tds_read_b64_tr_b16 v[74:75], v11 offset:19456\n\tv_add_f32 v204, v204, v209\n\tv_fma_f32 v172, v172, v172, 0.5\n\tv_sub_f32 v205, v205, v208\n\tv_exp_f32 v174, v126\n\tv_add_f32 v206, v206, v211\n\tds_read_b64_tr_b16 v[76:77], v11 offset:2048\n\tv_sub_f32 v207, v207, v210\n\tv_fma_f32 v173, v173, s69, s69\n\tv_cvt_pk_bf16_f32 v14, v204, v205\n\tglobal_load_lds_dwordx4 v6, s[98:99] offset:1024\n\tv_mfma_f32_32x32x16_bf16 v[108:123], v[48:51], a[12:15], v[108:123]\n\tv_rcp_f32 v172, v172\n\tv_cvt_pk_bf16_f32 v15, v206, v207\n\tds_read_b64_tr_b16 v[78:79], v11 offset:20480\n\tv_fma_f32 v173, v173, v173, 0.5\n\tbuffer_store_dwordx2 v[14:15], v18, s[56:59], s79 offen offset:64 sc1\n\tv_mfma_f32_32x32x16_bf16 v[92:107], v[52:55], a[16:19], v[92:107]\n\tv_exp_f32 v175, v127\n\tds_read_b64_tr_b16 v[80:81], v11 offset:3072\n\tv_fma_f32 v174, v174, s69, s69\n\tv_rcp_f32 v173, v173\n\ts_waitcnt lgkmcnt(10)\n\tv_mfma_f32_16x16x32_bf16 v[204:207], v[200:203], v[212:215], 0\n\tglobal_load_lds_dwordx4 v7, s[98:99] offset:2048\n\tv_mfma_f32_32x32x16_bf16 v[108:123], v[52:55], a[20:23], v[108:123]\n\tds_read_b64_tr_b16 v[82:83], v11 offset:21504\n\tv_fma_f32 v174, v174, v174, 0.5\n\tv_fma_f32 v172, -v124, v172, v124\n\tds_read_b64_tr_b16 v[84:85], v11 offset:4096\n\tv_exp_f32 v176, v128\n\tv_mfma_f32_16x16x32_bf16 v[208:211], v[200:203], v[216:219], 0\n\tv_fma_f32 v175, v175, s69, s69\n\tv_rcp_f32 v174, v174\n\tv_mfma_f32_32x32x16_bf16 v[92:107], v[56:59], a[24:27], v[92:107]\n\tds_read_b64_tr_b16 v[86:87], v11 offset:22528\n\ts_nop 3\n\tv_add_f32 v204, v204, v209\n\ts_add_u32 m0, s64, 15360\n\tv_sub_f32 v205, v205, v208\n\tv_fma_f32 v175, v175, v175, 0.5\n\tv_add_f32 v206, v206, v211\n\tv_fma_f32 v173, -v125, v173, v125\n\tv_sub_f32 v207, v207, v210\n\ts_add_u32 s98, s98, 0x6c000\n\ts_addc_u32 s99, s99, 0\n\tds_read_b64_tr_b16 v[88:89], v11 offset:5120\n\tv_exp_f32 v177, v129\n\tv_mfma_f32_32x32x16_bf16 v[108:123], v[56:59], a[28:31], v[108:123]\n\tds_read_b64_tr_b16 v[90:91], v11 offset:23552\n\tv_cvt_pk_bf16_f32 v16, v204, v205\n\tv_cvt_pk_bf16_f32 v17, v206, v207\n\tv_fma_f32 v176, v176, s69, s69\n\tv_rcp_f32 v175, v175\n\tbuffer_store_dwordx2 v[16:17], v18, s[56:59], s79 offen offset:96 sc1\n\tv_mfma_f32_32x32x16_bf16 v[92:107], v[60:63], a[32:35], v[92:107]\n\tglobal_load_lds_dwordx4 v5, s[98:99] offset:0\n\tv_fma_f32 v176, v176, v176, 0.5\n\tv_fma_f32 v174, -v126, v174, v126\n\tv_cvt_pk_bf16_f32 v188, v172, v173\n\ts_waitcnt lgkmcnt(9)\n\tv_mfma_f32_16x16x32_bf16 v[204:207], v[196:199], v[212:215], 0\n\tv_exp_f32 v178, v130\n\tv_mfma_f32_32x32x16_bf16 v[108:123], v[60:63], a[36:39], v[108:123]\n\tv_fma_f32 v177, v177, s69, s69\n\tglobal_load_lds_dwordx4 v6, s[98:99] offset:1024\n\tv_rcp_f32 v176, v176\n\tv_mfma_f32_16x16x32_bf16 v[208:211], v[196:199], v[216:219], 0\n\tv_fma_f32 v177, v177, v177, 0.5\n\tv_fma_f32 v175, -v127, v175, v127\n\tv_mfma_f32_32x32x16_bf16 v[92:107], v[64:67], a[40:43], v[92:107]\n\ts_nop 4\n\tv_add_f32 v204, v204, v209\n\tv_exp_f32 v179, v131\n\tv_sub_f32 v205, v205, v208\n\tv_add_f32 v206, v206, v211\n\tv_fma_f32 v178, v178, s69, s69\n\tv_sub_f32 v207, v207, v210\n\tglobal_load_lds_dwordx4 v7, s[98:99] offset:2048\n\tv_rcp_f32 v177, v177\n\tv_mfma_f32_32x32x16_bf16 v[108:123], v[64:67], a[44:47], v[108:123]\n\tv_cvt_pk_bf16_f32 v14, v204, v205\n\tv_cvt_pk_bf16_f32 v15, v206, v207\n\tv_fma_f32 v178, v178, v178, 0.5\n\tbuffer_store_dwordx2 v[14:15], v18, s[56:59], s79 offen offset:128 sc1\n\tv_fma_f32 v176, -v128, v176, v128\n\tv_cvt_pk_bf16_f32 v189, v174, v175\n\tv_exp_f32 v180, v132\n\ts_waitcnt vmcnt(10) lgkmcnt(0)\n\ts_barrier\n\tv_mfma_f32_32x32x16_bf16 v[92:107], v[68:71], a[48:51], v[92:107]\n\tds_read_b64_tr_b16 v[44:45], v11 offset:6144\n\ts_add_u32 m0, s64, 0\n\tv_fma_f32 v179, v179, s69, s69\n\tv_rcp_f32 v178, v178\n\tds_read_b64_tr_b16 v[46:47], v11 offset:24576\n\tv_lshrrev_b32 v220, 5, v0\n\tv_mul_u32_u24 v221, 18432, v220\n\tv_sub_u32 v13, v11, v221\n\tv_add_u32 v14, 73728, v13\n\tv_sub_u32 v15, v12, v221\n\tv_lshl_add_u32 v15, v220, 9, v15\n\tv_add_u32 v16, 36864, v15\n\tv_mfma_f32_32x32x16_bf16 v[108:123], v[68:71], a[52:55], v[108:123]\n\ts_add_u32 s98, s98, 0x6c000\n\ts_addc_u32 s99, s99, 0\n\tv_fma_f32 v179, v179, v179, 0.5\n\tds_read_b64_tr_b16 v[48:49], v11 offset:7168\n\tv_fma_f32 v177, -v129, v177, v129\n\tglobal_load_lds_dwordx4 v5, s[98:99] offset:0\n\tv_mfma_f32_32x32x16_bf16 v[92:107], v[72:75], a[56:59], v[92:107]\n\tv_exp_f32 v181, v133\n\tds_read_b64_tr_b16 v[50:51], v11 offset:25600\n\tv_fma_f32 v180, v180, s69, s69\n\tv_rcp_f32 v179, v179\n\tv_and_b32 v35, 31, v0\n\tv_lshrrev_b32 v35, 3, v35\n\tv_mul_u32_u24 v35, 73728, v35\n\tv_and_b32 v223, 7, v0\n\tv_lshl_add_u32 v35, v223, 5, v35\n\tv_lshrrev_b32 v223, 5, v0\n\tv_lshl_add_u32 v35, v223, 4, v35\n\ts_mul_i32 s95, s62, 512\n\tv_add_u32 v35, s95, v35\n\tv_mfma_f32_32x32x16_bf16 v[108:123], v[72:75], a[60:63], v[108:123]\n\tds_read_b64_tr_b16 v[52:53], v11 offset:8192\n\tv_fma_f32 v180, v180, v180, 0.5\n\tv_fma_f32 v178, -v130, v178, v130\n\tds_read_b64_tr_b16 v[54:55], v11 offset:26624\n\tv_cvt_pk_bf16_f32 v190, v176, v177\n\tglobal_load_lds_dwordx4 v6, s[98:99] offset:1024\n\tv_mfma_f32_32x32x16_bf16 v[92:107], v[76:79], a[64:67], v[92:107]\n\tv_exp_f32 v182, v134\n\tds_read_b64_tr_b16 v[56:57], v11 offset:9216\n\tv_fma_f32 v181, v181, s69, s69\n\tv_rcp_f32 v180, v180\n\tglobal_load_lds_dwordx4 v7, s[98:99] offset:2048\n\tv_mfma_f32_32x32x16_bf16 v[108:123], v[76:79], a[68:71], v[108:123]\n\tds_read_b64_tr_b16 v[58:59], v11 offset:27648\n\tv_fma_f32 v181, v181, v181, 0.5\n\tv_fma_f32 v179, -v131, v179, v131\n\tds_read_b64_tr_b16 v[60:61], v11 offset:10240\n\tv_exp_f32 v183, v135\n\ts_lshr_b32 s93, s62, 1\n\ts_mul_i32 s94, s93, 512\n\ts_add_u32 s95, s94, 4096\n\tv_add_u32 v36, s95, v35\n\ts_mul_i32 s95, s62, 512\n\tv_subrev_u32 v36, s95, v36\n\tv_mfma_f32_32x32x16_bf16 v[92:107], v[80:83], a[72:75], v[92:107]\n\tds_read_b64_tr_b16 v[62:63], v11 offset:28672\n\tv_fma_f32 v182, v182, s69, s69\n\tv_rcp_f32 v181, v181\n\ts_add_u32 m0, s64, 3072\n\tv_fma_f32 v182, v182, v182, 0.5\n\ts_add_u32 s98, s98, 0x6c000\n\ts_addc_u32 s99, s99, 0\n\tv_mfma_f32_32x32x16_bf16 v[108:123], v[80:83], a[76:79], v[108:123]\n\tds_read_b64_tr_b16 v[64:65], v11 offset:11264\n\tds_read_b64_tr_b16 v[66:67], v11 offset:29696\n\tv_fma_f32 v180, -v132, v180, v132\n\tv_cvt_pk_bf16_f32 v191, v178, v179\n\tv_exp_f32 v184, v136\n\ts_and_b32 s95, s62, 1\n\ts_mul_i32 s82, s95, 64\n\ts_and_b32 s94, s62, 1\n\ts_mul_i32 s95, s94, 64\n\ts_sub_u32 s83, 64, s95\n\ts_lshr_b32 s95, s62, 1\n\ts_mul_i32 s68, s95, 1024\n\tv_subrev_u32 v220, s68, v19\n\tv_xor_b32 v25, s82, v220\n\tv_xor_b32 v27, s83, v220\n\tv_subrev_u32 v220, s68, v20\n\tv_xor_b32 v26, s82, v220\n\tv_xor_b32 v28, s83, v220\n\tv_subrev_u32 v220, s68, v21\n\tv_xor_b32 v29, s82, v220\n\tv_xor_b32 v31, s83, v220\n\tv_subrev_u32 v220, s68, v22\n\tv_xor_b32 v30, s82, v220\n\tv_xor_b32 v32, s83, v220\n\tv_mfma_f32_32x32x16_bf16 v[92:107], v[84:87], a[80:83], v[92:107]\n\tglobal_load_lds_dwordx4 v5, s[98:99] offset:0\n\tv_fma_f32 v183, v183, s69, s69\n\tv_mfma_f32_32x32x16_bf16 v[108:123], v[84:87], a[84:87], v[108:123]\n\tv_rcp_f32 v182, v182\n\tv_fma_f32 v183, v183, v183, 0.5\n\tv_fma_f32 v181, -v133, v181, v133\n\tv_exp_f32 v185, v137\n\tv_mfma_f32_32x32x16_bf16 v[92:107], v[88:91], a[88:91], v[92:107]\n\tglobal_load_lds_dwordx4 v6, s[98:99] offset:1024\n\tv_fma_f32 v184, v184, s69, s69\n\tv_rcp_f32 v183, v183\n\tv_fma_f32 v184, v184, v184, 0.5\n\tv_mfma_f32_32x32x16_bf16 v[108:123], v[88:91], a[92:95], v[108:123]\n\tv_fma_f32 v182, -v134, v182, v134\n\tglobal_load_lds_dwordx4 v7, s[98:99] offset:2048\n\tv_cvt_pk_bf16_f32 v192, v180, v181\n\tv_exp_f32 v186, v138\n\tv_fma_f32 v185, v185, s69, s69\n\tv_rcp_f32 v184, v184\n\tds_write_b128 v19, v[188:191] offset:4096\n\ts_waitcnt vmcnt(7) lgkmcnt(1)\n\ts_barrier\n\tv_mfma_f32_32x32x16_bf16 v[92:107], v[44:47], a[96:99], v[92:107]\n\tds_read_b64_tr_b16 v[68:69], v11 offset:12288\n\ts_add_u32 m0, s64, 6144\n\tv_fma_f32 v185, v185, v185, 0.5\n\tv_fma_f32 v183, -v135, v183, v135\n\ts_add_u32 s98, s98, 0x6c000\n\ts_addc_u32 s99, s99, 0\n\tds_read_b64_tr_b16 v[70:71], v11 offset:30720\n\tv_mfma_f32_32x32x16_bf16 v[108:123], v[44:47], a[100:103], v[108:123]\n\tv_exp_f32 v187, v139\n\tds_read_b64_tr_b16 v[72:73], v11 offset:13312\n\tv_fma_f32 v186, v186, s69, s69\n\tv_rcp_f32 v185, v185\n\tglobal_load_lds_dwordx4 v5, s[98:99] offset:0\n\tv_mfma_f32_32x32x16_bf16 v[92:107], v[48:51], a[104:107], v[92:107]\n\tds_read_b64_tr_b16 v[74:75], v11 offset:31744\n\tv_fma_f32 v186, v186, v186, 0.5\n\tv_fma_f32 v184, -v136, v184, v136\n\tv_cvt_pk_bf16_f32 v193, v182, v183\n\tds_read_b64_tr_b16 v[76:77], v11 offset:14336\n\tv_mfma_f32_32x32x16_bf16 v[108:123], v[48:51], a[108:111], v[108:123]\n\tv_exp_f32 v172, v140\n\tglobal_load_lds_dwordx4 v6, s[98:99] offset:1024\n\tds_read_b64_tr_b16 v[78:79], v11 offset:32768\n\tv_mfma_f32_32x32x16_bf16 v[92:107], v[52:55], a[112:115], v[92:107]\n\tv_fma_f32 v187, v187, s69, s69\n\tv_rcp_f32 v186, v186\n\tds_read_b64_tr_b16 v[80:81], v11 offset:15360\n\tv_fma_f32 v187, v187, v187, 0.5\n\tv_fma_f32 v185, -v137, v185, v137\n\tv_exp_f32 v173, v141\n\tv_mfma_f32_32x32x16_bf16 v[108:123], v[52:55], a[116:119], v[108:123]\n\tds_read_b64_tr_b16 v[82:83], v11 offset:33792\n\tglobal_load_lds_dwordx4 v7, s[98:99] offset:2048\n\tv_fma_f32 v172, v172, s69, s69\n\tds_read_b64_tr_b16 v[84:85], v11 offset:16384\n\tv_mfma_f32_32x32x16_bf16 v[92:107], v[56:59], a[120:123], v[92:107]\n\tv_rcp_f32 v187, v187\n\tds_read_b64_tr_b16 v[86:87], v11 offset:34816\n\tv_fma_f32 v172, v172, v172, 0.5\n\tv_fma_f32 v186, -v138, v186, v138\n\ts_add_u32 m0, s64, 9216\n\tv_cvt_pk_bf16_f32 v194, v184, v185\n\tv_mfma_f32_32x32x16_bf16 v[108:123], v[56:59], a[124:127], v[108:123]\n\tds_read_b64_tr_b16 v[88:89], v11 offset:17408\n\tds_read_b64_tr_b16 v[90:91], v11 offset:35840\n\tv_exp_f32 v174, v142\n\ts_add_u32 s98, s98, 0x6c000\n\ts_addc_u32 s99, s99, 0\n\tv_fma_f32 v173, v173, s69, s69\n\tv_rcp_f32 v172, v172\n\tv_mfma_f32_32x32x16_bf16 v[92:107], v[60:63], a[128:131], v[92:107]\n\tglobal_load_lds_dwordx4 v5, s[98:99] offset:0\n\tv_fma_f32 v173, v173, v173, 0.5\n\tv_fma_f32 v187, -v139, v187, v139\n\tv_mfma_f32_32x32x16_bf16 v[108:123], v[60:63], a[132:135], v[108:123]\n\tv_exp_f32 v175, v143\n\tv_fma_f32 v174, v174, s69, s69\n\tv_rcp_f32 v173, v173\n\tv_fma_f32 v174, v174, v174, 0.5\n\tglobal_load_lds_dwordx4 v6, s[98:99] offset:1024\n\tv_mfma_f32_32x32x16_bf16 v[92:107], v[64:67], a[136:139], v[92:107]\n\tv_fma_f32 v172, -v140, v172, v140\n\tv_cvt_pk_bf16_f32 v195, v186, v187\n\tv_exp_f32 v176, v144\n\tv_fma_f32 v175, v175, s69, s69\n\tv_mfma_f32_32x32x16_bf16 v[108:123], v[64:67], a[140:143], v[108:123]\n\tglobal_load_lds_dwordx4 v7, s[98:99] offset:2048\n\tv_rcp_f32 v174, v174\n\tv_fma_f32 v175, v175, v175, 0.5\n\tv_fma_f32 v173, -v141, v173, v141\n\tds_write_b128 v20, v[192:195] offset:4096\n\ts_waitcnt vmcnt(6) lgkmcnt(1)\n\ts_barrier\n\ts_mul_i32 s93, s61, 256\n\ts_mul_i32 s92, s60, 8\n\ts_add_u32 s94, s93, s92\n\ts_add_u32 s95, s94, 2048\n\ts_add_u32 s83, s95, 24117248\n\tv_mov_b32 v222, s88\n\tv_mov_b32 v223, s89\n\tbuffer_store_dwordx2 v[222:223], v42, s[56:59], s83 offen sc1\n\tv_mfma_f32_32x32x16_bf16 v[92:107], v[68:71], a[144:147], v[92:107]\n\tds_read_b64_tr_b16 v[44:45], v11 offset:0\n\tv_mul_u32_u24 v8, 9216, v3\n\tv_exp_f32 v177, v145\n\tv_add_u32 v8, v8, v4\n\tv_fma_f32 v176, v176, s69, s69\n\tds_read_b64_tr_b16 v[46:47], v11 offset:18432\n\tv_add_u32 v9, 293888, v8\n\tv_mfma_f32_32x32x16_bf16 v[108:123], v[68:71], a[148:151], v[108:123]\n\tv_rcp_f32 v175, v175\n\tds_read_b64_tr_b16 v[48:49], v11 offset:1024\n\tv_add_u32 v10, 587776, v8\n\tv_fma_f32 v176, v176, v176, 0.5\n\ts_add_u32 m0, s64, 12288\n\tv_fma_f32 v174, -v142, v174, v142\n\tv_cvt_pk_bf16_f32 v188, v172, v173\n\tv_mfma_f32_32x32x16_bf16 v[92:107], v[72:75], a[152:155], v[92:107]\n\tds_read_b64_tr_b16 v[50:51], v11 offset:19456\n\ts_sub_u32 s98, s98, 0x4a3f00\n\ts_subb_u32 s99, s99, 0\n\tv_exp_f32 v178, v146\n\tv_fma_f32 v177, v177, s69, s69\n\tglobal_load_lds_dwordx4 v8, s[98:99] offset:0\n\tv_mfma_f32_32x32x16_bf16 v[108:123], v[72:75], a[156:159], v[108:123]\n\tds_read_b64_tr_b16 v[52:53], v11 offset:2048\n\tv_rcp_f32 v176, v176\n\tds_read_b64_tr_b16 v[54:55], v11 offset:20480\n\tv_fma_f32 v177, v177, v177, 0.5\n\tv_fma_f32 v175, -v143, v175, v143\n\tv_mfma_f32_32x32x16_bf16 v[92:107], v[76:79], a[160:163], v[92:107]\n\tds_read_b64_tr_b16 v[56:57], v11 offset:3072\n\tv_exp_f32 v179, v147\n\tv_fma_f32 v178, v178, s69, s69\n\tv_rcp_f32 v177, v177\n\tglobal_load_lds_dwordx4 v9, s[98:99] offset:1024\n\tv_mfma_f32_32x32x16_bf16 v[108:123], v[76:79], a[164:167], v[108:123]\n\tds_read_b64_tr_b16 v[58:59], v11 offset:21504\n\tv_fma_f32 v178, v178, v178, 0.5\n\tds_read_b64_tr_b16 v[60:61], v11 offset:4096\n\tv_fma_f32 v176, -v144, v176, v144\n\tv_cvt_pk_bf16_f32 v189, v174, v175\n\tglobal_load_lds_dwordx4 v10, s[98:99] offset:2048\n\tv_mfma_f32_32x32x16_bf16 v[92:107], v[80:83], a[168:171], v[92:107]\n\tds_read_b64_tr_b16 v[62:63], v11 offset:22528\n\tv_exp_f32 v180, v148\n\tv_fma_f32 v179, v179, s69, s69\n\tv_rcp_f32 v178, v178\n\tv_mfma_f32_32x32x16_bf16 v[108:123], v[80:83], a[172:175], v[108:123]\n\tds_read_b64_tr_b16 v[64:65], v11 offset:5120\n\tds_read_b64_tr_b16 v[66:67], v11 offset:23552\n\ts_add_u32 m0, s64, 15360\n\tv_fma_f32 v179, v179, v179, 0.5\n\tv_fma_f32 v177, -v145, v177, v145\n\ts_add_u32 s98, s98, 0xd8000\n\ts_addc_u32 s99, s99, 0\n\tv_mfma_f32_32x32x16_bf16 v[92:107], v[84:87], a[176:179], v[92:107]\n\tv_exp_f32 v181, v149\n\tv_fma_f32 v180, v180, s69, s69\n\tv_rcp_f32 v179, v179\n\tv_fma_f32 v180, v180, v180, 0.5\n\tglobal_load_lds_dwordx4 v8, s[98:99] offset:0\n\tv_mfma_f32_32x32x16_bf16 v[108:123], v[84:87], a[180:183], v[108:123]\n\tv_fma_f32 v178, -v146, v178, v146\n\tv_cvt_pk_bf16_f32 v190, v176, v177\n\tv_exp_f32 v182, v150\n\tv_fma_f32 v181, v181, s69, s69\n\tglobal_load_lds_dwordx4 v9, s[98:99] offset:1024\n\tv_mfma_f32_32x32x16_bf16 v[92:107], v[88:91], a[184:187], v[92:107]\n\tv_rcp_f32 v180, v180\n\tv_fma_f32 v181, v181, v181, 0.5\n\tv_fma_f32 v179, -v147, v179, v147\n\tv_exp_f32 v183, v151\n\tv_mfma_f32_32x32x16_bf16 v[108:123], v[88:91], a[188:191], v[108:123]\n\tglobal_load_lds_dwordx4 v10, s[98:99] offset:2048\n\tv_fma_f32 v182, v182, s69, s69\n\tv_rcp_f32 v181, v181\n\ts_waitcnt vmcnt(7) lgkmcnt(0)\n\ts_barrier\n\tv_mfma_f32_32x32x16_bf16 v[92:107], v[44:47], a[192:195], v[92:107]\n\tds_read_b64_tr_b16 v[68:69], v11 offset:6144\n\ts_add_u32 m0, s64, 0\n\tv_fma_f32 v182, v182, v182, 0.5\n\tv_fma_f32 v180, -v148, v180, v148\n\ts_add_u32 s98, s98, 0xd8000\n\ts_addc_u32 s99, s99, 0\n\tds_read_b64_tr_b16 v[70:71], v11 offset:24576\n\tv_mfma_f32_32x32x16_bf16 v[108:123], v[44:47], a[196:199], v[108:123]\n\tv_cvt_pk_bf16_f32 v191, v178, v179\n\tv_exp_f32 v184, v152\n\tds_read_b64_tr_b16 v[72:73], v11 offset:7168\n\tv_fma_f32 v183, v183, s69, s69\n\tglobal_load_lds_dwordx4 v8, s[98:99] offset:0\n\tv_mfma_f32_32x32x16_bf16 v[92:107], v[48:51], a[200:203], v[92:107]\n\tds_read_b64_tr_b16 v[74:75], v11 offset:25600\n\tv_rcp_f32 v182, v182\n\tds_read_b128 v[156:159], v34 offset:0\n\tv_fma_f32 v183, v183, v183, 0.5\n\tv_fma_f32 v181, -v149, v181, v149\n\tv_mfma_f32_32x32x16_bf16 v[108:123], v[48:51], a[204:207], v[108:123]\n\tds_read_b64_tr_b16 v[76:77], v11 offset:8192\n\tv_exp_f32 v185, v153\n\tds_read_b64_tr_b16 v[78:79], v11 offset:26624\n\tv_fma_f32 v184, v184, s69, s69\n\tglobal_load_lds_dwordx4 v9, s[98:99] offset:1024\n\tv_mfma_f32_32x32x16_bf16 v[92:107], v[52:55], a[208:211], v[92:107]\n\tds_read_b64_tr_b16 v[80:81], v11 offset:9216\n\tv_rcp_f32 v183, v183\n\tv_fma_f32 v184, v184, v184, 0.5\n\tv_fma_f32 v182, -v150, v182, v150\n\tds_read_b128 v[160:163], v34 offset:32\n\tv_mfma_f32_32x32x16_bf16 v[108:123], v[52:55], a[212:215], v[108:123]\n\tds_read_b64_tr_b16 v[82:83], v11 offset:27648\n\tglobal_load_lds_dwordx4 v10, s[98:99] offset:2048\n\tv_cvt_pk_bf16_f32 v192, v180, v181\n\tv_exp_f32 v186, v154\n\tv_mfma_f32_32x32x16_bf16 v[92:107], v[56:59], a[216:219], v[92:107]\n\tds_read_b64_tr_b16 v[84:85], v11 offset:10240\n\tv_fma_f32 v185, v185, s69, s69\n\tds_read_b64_tr_b16 v[86:87], v11 offset:28672\n\tv_rcp_f32 v184, v184\n\ts_add_u32 m0, s64, 3072\n\tv_fma_f32 v185, v185, v185, 0.5\n\tv_mfma_f32_32x32x16_bf16 v[108:123], v[56:59], a[220:223], v[108:123]\n\tds_read_b64_tr_b16 v[88:89], v11 offset:11264\n\tds_read_b64_tr_b16 v[90:91], v11 offset:29696\n\ts_add_u32 s98, s98, 0xd8000\n\ts_addc_u32 s99, s99, 0\n\tv_fma_f32 v183, -v151, v183, v151\n\tds_read_b128 v[164:167], v34 offset:64\n\tv_exp_f32 v187, v155\n\tv_mfma_f32_32x32x16_bf16 v[92:107], v[60:63], a[224:227], v[92:107]\n\tglobal_load_lds_dwordx4 v8, s[98:99] offset:0\n\tv_fma_f32 v186, v186, s69, s69\n\tv_rcp_f32 v185, v185\n\tv_mfma_f32_32x32x16_bf16 v[108:123], v[60:63], a[228:231], v[108:123]\n\tv_fma_f32 v186, v186, v186, 0.5\n\tv_fma_f32 v184, -v152, v184, v152\n\tv_cvt_pk_bf16_f32 v193, v182, v183\n\tv_fma_f32 v187, v187, s69, s69\n\tv_rcp_f32 v186, v186\n\tglobal_load_lds_dwordx4 v9, s[98:99] offset:1024\n\tv_mfma_f32_32x32x16_bf16 v[92:107], v[64:67], a[232:235], v[92:107]\n\tds_read_b128 v[168:171], v34 offset:96\n\tv_fma_f32 v187, v187, v187, 0.5\n\tv_fma_f32 v185, -v153, v185, v153\n\tv_rcp_f32 v187, v187\n\tv_fma_f32 v186, -v154, v186, v154\n\tv_mfma_f32_32x32x16_bf16 v[108:123], v[64:67], a[236:239], v[108:123]\n\tglobal_load_lds_dwordx4 v10, s[98:99] offset:2048\n\tv_cvt_pk_bf16_f32 v194, v184, v185\n\tv_fma_f32 v187, -v155, v187, v155\n\tv_cvt_pk_bf16_f32 v195, v186, v187\n\tds_write_b128 v21, v[188:191] offset:4096\n\tds_write_b128 v22, v[192:195] offset:4096\n\ts_waitcnt vmcnt(6) lgkmcnt(4)\n\ts_barrier\n\tv_mfma_f32_32x32x16_bf16 v[92:107], v[68:71], a[240:243], v[92:107]\n\tds_read_b64_tr_b16 v[44:45], v12 offset:12288\n\ts_add_u32 m0, s64, 6144\n\ts_add_u32 s98, s98, 0xd8000\n\ts_addc_u32 s99, s99, 0\n\tv_mfma_f32_32x32x16_bf16 v[108:123], v[68:71], a[244:247], v[108:123]\n\tds_read_b64_tr_b16 v[46:47], v12 offset:12800\n\tds_read_b64_tr_b16 v[48:49], v12 offset:49152\n\tglobal_load_lds_dwordx4 v8, s[98:99] offset:0\n\tv_mfma_f32_32x32x16_bf16 v[92:107], v[72:75], a[248:251], v[92:107]\n\tds_read_b64_tr_b16 v[50:51], v12 offset:49664\n\tds_read_b64_tr_b16 v[52:53], v12 offset:13312\n\tv_mfma_f32_32x32x16_bf16 v[108:123], v[72:75], a[252:255], v[108:123]\n\tds_read_b64_tr_b16 v[54:55], v12 offset:13824\n\tds_read_b64_tr_b16 v[56:57], v12 offset:50176\n\tv_mfma_f32_32x32x16_bf16 v[92:107], v[76:79], v[224:227], v[92:107]\n\tds_read_b64_tr_b16 v[58:59], v12 offset:50688\n\tglobal_load_lds_dwordx4 v9, s[98:99] offset:1024\n\tv_mfma_f32_32x32x16_bf16 v[108:123], v[76:79], v[228:231], v[108:123]\n\tds_read_b64_tr_b16 v[60:61], v12 offset:14336\n\tv_mfma_f32_32x32x16_bf16 v[92:107], v[80:83], v[232:235], v[92:107]\n\tds_read_b64_tr_b16 v[62:63], v12 offset:14848\n\tds_read_b64_tr_b16 v[64:65], v12 offset:51200\n\tglobal_load_lds_dwordx4 v10, s[98:99] offset:2048\n\tv_mfma_f32_32x32x16_bf16 v[108:123], v[80:83], v[236:239], v[108:123]\n\tds_read_b64_tr_b16 v[66:67], v12 offset:51712\n\tds_read_b64_tr_b16 v[68:69], v12 offset:15360\n\tv_mfma_f32_32x32x16_bf16 v[92:107], v[84:87], v[240:243], v[92:107]\n\tds_read_b64_tr_b16 v[70:71], v12 offset:15872\n\tv_mfma_f32_32x32x16_bf16 v[108:123], v[84:87], v[244:247], v[108:123]\n\tds_read_b64_tr_b16 v[72:73], v12 offset:52224\n\ts_add_u32 m0, s64, 9216\n\ts_add_u32 s98, s98, 0xd8000\n\ts_addc_u32 s99, s99, 0\n\tv_mfma_f32_32x32x16_bf16 v[92:107], v[88:91], v[248:251], v[92:107]\n\tds_read_b64_tr_b16 v[74:75], v12 offset:52736\n\tds_read_b64_tr_b16 v[76:77], v12 offset:16384\n\tglobal_load_lds_dwordx4 v8, s[98:99] offset:0\n\tv_mfma_f32_32x32x16_bf16 v[108:123], v[88:91], v[252:255], v[108:123]\n\tds_read_b64_tr_b16 v[78:79], v12 offset:16896\n\tds_read_b64_tr_b16 v[80:81], v12 offset:53248\n\ts_waitcnt lgkmcnt(15)\n\tv_mfma_f32_32x32x16_bf16 v[124:139], v[44:47], a[0:3], v[156:171]\n\tds_read_b64_tr_b16 v[82:83], v12 offset:53760\n\tds_read_b64_tr_b16 v[84:85], v12 offset:17408\n\tv_mfma_f32_32x32x16_bf16 v[124:139], v[48:51], a[8:11], v[124:139]\n\tds_read_b64_tr_b16 v[86:87], v12 offset:17920\n\tv_exp_f32 v172, v92\n\tv_exp_f32 v173, v93\n\tv_fma_f32 v172, v172, s69, s69\n\tv_fma_f32 v172, v172, v172, 0.5\n\tv_exp_f32 v174, v94\n\tv_fma_f32 v173, v173, s69, s69\n\tv_rcp_f32 v172, v172\n\tglobal_load_lds_dwordx4 v9, s[98:99] offset:1024\n\tv_fma_f32 v173, v173, v173, 0.5\n\tv_exp_f32 v175, v95\n\ts_waitcnt lgkmcnt(15)\n\tv_mfma_f32_32x32x16_bf16 v[124:139], v[52:55], a[16:19], v[124:139]\n\tds_read_b64_tr_b16 v[88:89], v12 offset:54272\n\tv_fma_f32 v174, v174, s69, s69\n\tv_rcp_f32 v173, v173\n\tv_fma_f32 v174, v174, v174, 0.5\n\tv_fma_f32 v172, -v92, v172, v92\n\tv_exp_f32 v176, v96\n\tv_fma_f32 v175, v175, s69, s69\n\tv_rcp_f32 v174, v174\n\tv_fma_f32 v175, v175, v175, 0.5\n\tv_fma_f32 v173, -v93, v173, v93\n\ts_waitcnt lgkmcnt(15)\n\tv_mfma_f32_32x32x16_bf16 v[124:139], v[56:59], a[24:27], v[124:139]\n\tds_read_b64_tr_b16 v[90:91], v12 offset:54784\n\tv_exp_f32 v177, v97\n\tv_fma_f32 v176, v176, s69, s69\n\tv_rcp_f32 v175, v175\n\tv_fma_f32 v176, v176, v176, 0.5\n\tv_fma_f32 v174, -v94, v174, v94\n\tv_cvt_pk_bf16_f32 v188, v172, v173\n\tv_exp_f32 v178, v98\n\tv_fma_f32 v177, v177, s69, s69\n\tglobal_load_lds_dwordx4 v10, s[98:99] offset:2048\n\tv_rcp_f32 v176, v176\n\ts_waitcnt lgkmcnt(14)\n\tv_mfma_f32_32x32x16_bf16 v[124:139], v[60:63], a[32:35], v[124:139]\n\tv_fma_f32 v177, v177, v177, 0.5\n\tv_fma_f32 v175, -v95, v175, v95\n\tv_exp_f32 v179, v99\n\tv_fma_f32 v178, v178, s69, s69\n\tv_rcp_f32 v177, v177\n\tv_fma_f32 v178, v178, v178, 0.5\n\tv_fma_f32 v176, -v96, v176, v96\n\tv_cvt_pk_bf16_f32 v189, v174, v175\n\tv_exp_f32 v180, v100\n\ts_waitcnt lgkmcnt(12)\n\tv_mfma_f32_32x32x16_bf16 v[124:139], v[64:67], a[40:43], v[124:139]\n\tv_fma_f32 v179, v179, s69, s69\n\tv_rcp_f32 v178, v178\n\tv_fma_f32 v179, v179, v179, 0.5\n\tv_fma_f32 v177, -v97, v177, v97\n\tv_exp_f32 v181, v101\n\tv_fma_f32 v180, v180, s69, s69\n\tv_rcp_f32 v179, v179\n\tv_fma_f32 v180, v180, v180, 0.5\n\tv_fma_f32 v178, -v98, v178, v98\n\ts_waitcnt vmcnt(6) lgkmcnt(0)\n\ts_barrier\n\tv_mfma_f32_32x32x16_bf16 v[124:139], v[68:71], a[48:51], v[124:139]\n\tds_read_b64_tr_b16 v[44:45], v12 offset:0\n\tds_read_b64_tr_b16 v[48:49], v12 offset:36864\n\tv_mul_u32_u24 v5, 1152, v1\n\tv_cvt_pk_bf16_f32 v190, v176, v177\n\tds_read_b128 a[0:3], v25 offset:0\n\tv_add_u32 v5, v5, v2\n\tv_exp_f32 v182, v102\n\tv_fma_f32 v181, v181, s69, s69\n\tv_add_u32 v6, 17408, v5\n\tds_read_b128 a[4:7], v29 offset:0\n\tv_rcp_f32 v180, v180\n\tv_mfma_f32_32x32x16_bf16 v[124:139], v[72:75], a[56:59], v[124:139]\n\tds_read_b64_tr_b16 v[46:47], v12 offset:512\n\tds_read_b64_tr_b16 v[50:51], v12 offset:37376\n\tds_read_b64_tr_b16 v[52:53], v12 offset:1024\n\ts_mul_i32 s94, s61, 256\n\ts_add_u32 s95, s94, 2048\n\ts_add_u32 s78, s95, 24117248\n\tv_add_u32 v7, 34816, v5\n\tv_fma_f32 v181, v181, v181, 0.5\n\ts_add_u32 m0, s64, 12288\n\tv_fma_f32 v179, -v99, v179, v99\n\tds_read_b128 a[8:11], v26 offset:0\n\tv_mfma_f32_32x32x16_bf16 v[124:139], v[76:79], a[64:67], v[124:139]\n\tds_read_b64_tr_b16 v[54:55], v12 offset:1536\n\tds_read_b64_tr_b16 v[56:57], v12 offset:37888\n\tv_and_b32 v210, 31, v0\n\tv_exp_f32 v183, v103\n\tv_fma_f32 v182, v182, s69, s69\n\ts_add_u32 s98, s81, 0x0\n\ts_add_u32 s98, s98, s48\n\ts_addc_u32 s99, s49, 0\n\tv_rcp_f32 v181, v181\n\tds_read_b128 a[12:15], v30 offset:0\n\tv_mfma_f32_32x32x16_bf16 v[124:139], v[80:83], a[72:75], v[124:139]\n\tds_read_b64_tr_b16 v[58:59], v12 offset:38400\n\tds_read_b64_tr_b16 v[60:61], v12 offset:2048\n\tv_lshlrev_b32 v210, 3, v210\n\tv_fma_f32 v182, v182, v182, 0.5\n\tv_fma_f32 v180, -v100, v180, v100\n\tv_cvt_pk_bf16_f32 v191, v178, v179\n\tbuffer_load_dwordx2 v[208:209], v210, s[56:59], s78 offen sc1\n\tglobal_load_lds_dwordx4 v5, s[98:99] offset:0\n\tv_mfma_f32_32x32x16_bf16 v[124:139], v[84:87], a[80:83], v[124:139]\n\tds_read_b64_tr_b16 v[62:63], v12 offset:2560\n\tds_read_b64_tr_b16 v[64:65], v12 offset:38912\n\tds_read_b128 a[16:19], v27 offset:0\n\tv_exp_f32 v184, v104\n\tv_fma_f32 v183, v183, s69, s69\n\tds_read_b128 a[20:23], v31 offset:0\n\tv_rcp_f32 v182, v182\n\tglobal_load_lds_dwordx4 v6, s[98:99] offset:1024\n\tv_mfma_f32_32x32x16_bf16 v[124:139], v[88:91], a[88:91], v[124:139]\n\tds_read_b64_tr_b16 v[66:67], v12 offset:39424\n\tds_read_b64_tr_b16 v[68:69], v12 offset:3072\n\tv_fma_f32 v183, v183, v183, 0.5\n\tv_fma_f32 v181, -v101, v181, v101\n\tds_read_b128 a[24:27], v28 offset:0\n\tds_read_b128 a[28:31], v32 offset:0\n\tv_exp_f32 v185, v105\n\tv_fma_f32 v184, v184, s69, s69\n\ts_waitcnt lgkmcnt(15)\n\tv_mfma_f32_32x32x16_bf16 v[124:139], v[44:47], a[96:99], v[124:139]\n\tds_read_b64_tr_b16 v[70:71], v12 offset:3584\n\tds_read_b64_tr_b16 v[72:73], v12 offset:39936\n\tds_read_b64_tr_b16 v[76:77], v12 offset:4096\n\tv_rcp_f32 v183, v183\n\tv_fma_f32 v184, v184, v184, 0.5\n\tglobal_load_lds_dwordx4 v7, s[98:99] offset:2048\n\tv_fma_f32 v182, -v102, v182, v102\n\tds_read_b128 a[32:35], v25 offset:1024\n\tv_mfma_f32_32x32x16_bf16 v[124:139], v[48:51], a[104:107], v[124:139]\n\tds_read_b64_tr_b16 v[74:75], v12 offset:40448\n\tds_read_b64_tr_b16 v[78:79], v12 offset:4608\n\tds_read_b64_tr_b16 v[80:81], v12 offset:40960\n\tv_cvt_pk_bf16_f32 v192, v180, v181\n\tv_exp_f32 v186, v106\n\tv_fma_f32 v185, v185, s69, s69\n\tds_read_b128 a[36:39], v29 offset:1024\n\tv_rcp_f32 v184, v184\n\ts_add_u32 m0, s64, 15360\n\tv_fma_f32 v185, v185, v185, 0.5\n\ts_waitcnt lgkmcnt(15)\n\tv_mfma_f32_32x32x16_bf16 v[124:139], v[52:55], a[112:115], v[124:139]\n\tds_read_b64_tr_b16 v[82:83], v12 offset:41472\n\tds_read_b64_tr_b16 v[84:85], v12 offset:5120\n\ts_add_u32 s98, s98, 0xd800\n\ts_addc_u32 s99, s99, 0\n\tv_fma_f32 v183, -v103, v183, v103\n\tds_read_b128 a[40:43], v26 offset:1024\n\tv_exp_f32 v187, v107\n\tglobal_load_lds_dwordx4 v5, s[98:99] offset:0\n\tv_mfma_f32_32x32x16_bf16 v[124:139], v[56:59], a[120:123], v[124:139]\n\tds_read_b64_tr_b16 v[86:87], v12 offset:5632\n\tds_read_b64_tr_b16 v[88:89], v12 offset:41984\n\tv_fma_f32 v186, v186, s69, s69\n\tv_rcp_f32 v185, v185\n\tds_read_b128 a[44:47], v30 offset:1024\n\tv_fma_f32 v186, v186, v186, 0.5\n\tv_fma_f32 v184, -v104, v184, v104\n\tds_read_b128 a[52:55], v31 offset:1024\n\tv_cvt_pk_bf16_f32 v193, v182, v183\n\tv_exp_f32 v172, v108\n\tv_mfma_f32_32x32x16_bf16 v[124:139], v[60:63], a[128:131], v[124:139]\n\tds_read_b64_tr_b16 v[90:91], v12 offset:42496\n\tglobal_load_lds_dwordx4 v6, s[98:99] offset:1024\n\tv_fma_f32 v187, v187, s69, s69\n\tds_read_b128 a[60:63], v32 offset:1024\n\tv_rcp_f32 v186, v186\n\tv_fma_f32 v187, v187, v187, 0.5\n\tv_fma_f32 v185, -v105, v185, v105\n\tds_read_b128 a[68:71], v29 offset:2048\n\ts_waitcnt lgkmcnt(15)\n\tv_mfma_f32_32x32x16_bf16 v[124:139], v[64:67], a[136:139], v[124:139]\n\tv_exp_f32 v173, v109\n\tglobal_load_lds_dwordx4 v7, s[98:99] offset:2048\n\tv_fma_f32 v172, v172, s69, s69\n\tv_rcp_f32 v187, v187\n\tds_read_b128 a[76:79], v30 offset:2048\n\tv_fma_f32 v172, v172, v172, 0.5\n\tv_fma_f32 v186, -v106, v186, v106\n\tds_write_b128 v19, v[188:191] offset:6144\n\ts_waitcnt vmcnt(7) lgkmcnt(4)\n\ts_barrier\n\tv_mfma_f32_32x32x16_bf16 v[124:139], v[68:71], a[144:147], v[124:139]\n\tds_read_b64_tr_b16 v[44:45], v12 offset:6144\n\tds_read_b64_tr_b16 v[48:49], v12 offset:43008\n\ts_add_u32 m0, s64, 0\n\tv_cvt_pk_bf16_f32 v194, v184, v185\n\tds_read_b128 a[84:87], v31 offset:2048\n\tds_read_b128 a[92:95], v32 offset:2048\n\tv_exp_f32 v174, v110\n\ts_add_u32 s98, s98, 0xd800\n\ts_addc_u32 s99, s99, 0\n\tv_mfma_f32_32x32x16_bf16 v[124:139], v[72:75], a[152:155], v[124:139]\n\tds_read_b64_tr_b16 v[46:47], v12 offset:6656\n\tds_read_b64_tr_b16 v[50:51], v12 offset:43520\n\tds_read_b64_tr_b16 v[52:53], v12 offset:7168\n\tv_fma_f32 v173, v173, s69, s69\n\tv_rcp_f32 v172, v172\n\tglobal_load_lds_dwordx4 v5, s[98:99] offset:0\n\tv_fma_f32 v173, v173, v173, 0.5\n\tv_mfma_f32_32x32x16_bf16 v[124:139], v[76:79], a[160:163], v[124:139]\n\tds_read_b64_tr_b16 v[54:55], v12 offset:7680\n\tds_read_b64_tr_b16 v[56:57], v12 offset:44032\n\tv_fma_f32 v187, -v107, v187, v107\n\tds_read_b128 a[100:103], v29 offset:3072\n\tv_exp_f32 v175, v111\n\tv_fma_f32 v174, v174, s69, s69\n\tv_rcp_f32 v173, v173\n\tds_read_b128 a[108:111], v30 offset:3072\n\tv_fma_f32 v174, v174, v174, 0.5\n\tv_mfma_f32_32x32x16_bf16 v[124:139], v[80:83], a[168:171], v[124:139]\n\tds_read_b64_tr_b16 v[58:59], v12 offset:44544\n\tds_read_b64_tr_b16 v[60:61], v12 offset:8192\n\tglobal_load_lds_dwordx4 v6, s[98:99] offset:1024\n\tv_fma_f32 v172, -v108, v172, v108\n\tv_cvt_pk_bf16_f32 v195, v186, v187\n\tds_read_b128 a[116:119], v31 offset:3072\n\tv_exp_f32 v176, v112\n\tv_mfma_f32_32x32x16_bf16 v[124:139], v[84:87], a[176:179], v[124:139]\n\tds_read_b64_tr_b16 v[62:63], v12 offset:8704\n\tds_read_b64_tr_b16 v[64:65], v12 offset:45056\n\tv_fma_f32 v175, v175, s69, s69\n\tds_read_b128 a[124:127], v32 offset:3072\n\tv_rcp_f32 v174, v174\n\tv_fma_f32 v175, v175, v175, 0.5\n\tglobal_load_lds_dwordx4 v7, s[98:99] offset:2048\n\tv_mfma_f32_32x32x16_bf16 v[124:139], v[88:91], a[184:187], v[124:139]\n\tds_read_b64_tr_b16 v[66:67], v12 offset:45568\n\tds_read_b64_tr_b16 v[68:69], v12 offset:9216\n\tv_fma_f32 v173, -v109, v173, v109\n\tds_read_b128 a[132:135], v29 offset:4096\n\tv_exp_f32 v177, v113\n\tv_fma_f32 v176, v176, s69, s69\n\tv_rcp_f32 v175, v175\n\tds_read_b128 a[140:143], v30 offset:4096\n\tv_fma_f32 v176, v176, v176, 0.5\n\ts_waitcnt lgkmcnt(15)\n\tv_mfma_f32_32x32x16_bf16 v[124:139], v[44:47], a[192:195], v[124:139]\n\tds_read_b64_tr_b16 v[70:71], v12 offset:9728\n\tds_read_b64_tr_b16 v[72:73], v12 offset:46080\n\tds_read_b64_tr_b16 v[76:77], v12 offset:10240\n\tv_fma_f32 v174, -v110, v174, v110\n\tds_read_b128 a[148:151], v31 offset:4096\n\ts_add_u32 m0, s64, 3072\n\tv_cvt_pk_bf16_f32 v188, v172, v173\n\tv_exp_f32 v178, v114\n\ts_add_u32 s98, s98, 0xd800\n\ts_addc_u32 s99, s99, 0\n\tv_mfma_f32_32x32x16_bf16 v[124:139], v[48:51], a[200:203], v[124:139]\n\tds_read_b64_tr_b16 v[74:75], v12 offset:46592\n\tds_read_b64_tr_b16 v[78:79], v12 offset:10752\n\tds_read_b64_tr_b16 v[80:81], v12 offset:47104\n\tv_fma_f32 v177, v177, s69, s69\n\tds_read_b128 a[156:159], v32 offset:4096\n\tds_read_b128 a[164:167], v29 offset:5120\n\tv_rcp_f32 v176, v176\n\tglobal_load_lds_dwordx4 v5, s[98:99] offset:0\n\ts_waitcnt lgkmcnt(15)\n\tv_mfma_f32_32x32x16_bf16 v[124:139], v[52:55], a[208:211], v[124:139]\n\tds_read_b64_tr_b16 v[82:83], v12 offset:47616\n\tds_read_b64_tr_b16 v[84:85], v12 offset:11264\n\tv_fma_f32 v177, v177, v177, 0.5\n\tv_fma_f32 v175, -v111, v175, v111\n\tv_exp_f32 v179, v115\n\tv_fma_f32 v178, v178, s69, s69\n\tv_rcp_f32 v177, v177\n\tv_mfma_f32_32x32x16_bf16 v[124:139], v[56:59], a[216:219], v[124:139]\n\tds_read_b64_tr_b16 v[86:87], v12 offset:11776\n\tds_read_b64_tr_b16 v[88:89], v12 offset:48128\n\tds_read_b128 a[172:175], v30 offset:5120\n\tv_fma_f32 v178, v178, v178, 0.5\n\tv_fma_f32 v176, -v112, v176, v112\n\tds_read_b128 a[180:183], v31 offset:5120\n\tglobal_load_lds_dwordx4 v6, s[98:99] offset:1024\n\tv_cvt_pk_bf16_f32 v189, v174, v175\n\tv_mfma_f32_32x32x16_bf16 v[124:139], v[60:63], a[224:227], v[124:139]\n\tds_read_b64_tr_b16 v[90:91], v12 offset:48640\n\tv_exp_f32 v180, v116\n\tv_fma_f32 v179, v179, s69, s69\n\tds_read_b128 a[188:191], v32 offset:5120\n\tds_read_b128 a[48:51], v27 offset:1024\n\tv_rcp_f32 v178, v178\n\tv_fma_f32 v179, v179, v179, 0.5\n\tglobal_load_lds_dwordx4 v7, s[98:99] offset:2048\n\ts_waitcnt lgkmcnt(15)\n\tv_mfma_f32_32x32x16_bf16 v[124:139], v[64:67], a[232:235], v[124:139]\n\tv_fma_f32 v177, -v113, v177, v113\n\tv_exp_f32 v181, v117\n\tv_fma_f32 v180, v180, s69, s69\n\tv_rcp_f32 v179, v179\n\tds_read_b128 a[56:59], v28 offset:1024\n\tv_fma_f32 v180, v180, v180, 0.5\n\tv_fma_f32 v178, -v114, v178, v114\n\tds_write_b128 v20, v[192:195] offset:6144\n\ts_waitcnt vmcnt(6) lgkmcnt(4)\n\ts_barrier\n\tv_cmp_ne_u64 vcc, s[88:89], v[208:209]\n\ts_nop 3\n\ts_cbranch_vccz L_ok_v_%=\n\ts_mov_b32 s83, 0\n\tL_sp_v_%=:\n\ts_sleep 1\n\tbuffer_load_dwordx2 v[208:209], v210, s[56:59], s78 offen sc1\n\ts_waitcnt vmcnt(0)\n\tv_cmp_ne_u64 vcc, s[88:89], v[208:209]\n\ts_add_u32 s83, s83, 1\n\ts_nop 2\n\ts_cbranch_vccz L_ok_v_%=\n\ts_cmp_lt_u32 s83, 16384\n\ts_cbranch_scc1 L_sp_v_%=\n\tL_ok_v_%=:\n\tv_mfma_f32_32x32x16_bf16 v[124:139], v[68:71], a[240:243], v[124:139]\n\tds_read_b64_tr_b16 v[44:45], v13 offset:12288\n\tds_read_b64_tr_b16 v[46:47], v13 offset:49152\n\ts_add_u32 m0, s64, 6144\n\tv_cvt_pk_bf16_f32 v190, v176, v177\n\tds_read_b128 a[64:67], v25 offset:2048\n\tds_read_b128 a[72:75], v26 offset:2048\n\tv_and_b32 v212, 15, v0\n\ts_mul_i32 s95, s62, 16\n\tv_add_u32 v212, s95, v212\n\tv_lshl_add_u32 v212, v212, 5, s60\n\tv_lshrrev_b32 v217, 4, v0\n\tv_lshlrev_b32 v217, 3, v217\n\tv_mul_u32_u24 v212, v212, v217\n\ts_mul_i32 s95, s62, 1024\n\tv_lshl_add_u32 v196, v0, 4, s95\n\tv_exp_f32 v182, v118\n\ts_add_u32 s98, s98, 0xd800\n\ts_addc_u32 s99, s99, 0\n\tv_fma_f32 v181, v181, s69, s69\n\tv_rcp_f32 v180, v180\n\tds_read_b64_tr_b16 v[48:49], v13 offset:13312\n\tv_add_u32 v197, 150016, v196\n\tglobal_load_lds_dwordx4 v5, s[98:99] offset:0\n\tv_fma_f32 v181, v181, v181, 0.5\n\tv_fma_f32 v179, -v115, v179, v115\n\tv_mfma_f32_32x32x16_bf16 v[124:139], v[72:75], a[248:251], v[124:139]\n\tds_read_b64_tr_b16 v[50:51], v13 offset:50176\n\tds_read_b128 a[80:83], v27 offset:2048\n\ts_mul_i32 s93, s61, 32\n\ts_add_u32 s94, s93, s60\n\ts_mul_i32 s95, s94, 10240\n\ts_add_u32 s79, s95, 21495808\n\tv_exp_f32 v183, v119\n\tv_and_b32 v213, 15, v0\n\ts_mul_i32 s95, s62, 16\n\tv_add_u32 v213, s95, v213\n\tv_lshl_add_u32 v213, v213, 5, s60\n\tds_read_b64_tr_b16 v[52:53], v13 offset:14336\n\tv_fma_f32 v182, v182, s69, s69\n\tds_read_b128 a[88:91], v28 offset:2048\n\tv_rcp_f32 v181, v181\n\tv_fma_f32 v182, v182, v182, 0.5\n\tglobal_load_lds_dwordx4 v6, s[98:99] offset:1024\n\tv_cvt_f32_u32 v212, v212\n\tv_fma_f32 v180, -v116, v180, v116\n\tds_read_b64_tr_b16 v[54:55], v13 offset:51200\n\tds_read_b128 a[96:99], v25 offset:3072\n\tv_cvt_f32_u32 v213, v213\n\tv_cvt_pk_bf16_f32 v191, v178, v179\n\tv_exp_f32 v184, v120\n\tv_mul_f32 v212, 0x3a000000, v212\n\tv_mfma_f32_32x32x16_bf16 v[124:139], v[76:79], v[224:227], v[124:139]\n\tds_read_b64_tr_b16 v[56:57], v13 offset:15360\n\tv_mul_f32 v213, 0x3a000000, v213\n\tds_read_b128 a[104:107], v26 offset:3072\n\tv_fma_f32 v183, v183, s69, s69\n\tv_fract_f32 v214, v213\n\tds_read_b64_tr_b16 v[58:59], v13 offset:52224\n\tbuffer_load_dwordx4 v[200:203], v196, s[56:59], s79 offen sc1\n\ts_add_u32 s79, s79, 4096\n\tv_rcp_f32 v182, v182\n\tv_cos_f32 v214, v214\n\tglobal_load_lds_dwordx4 v7, s[98:99] offset:2048\n\tv_fma_f32 v183, v183, v183, 0.5\n\tds_read_b128 a[112:115], v27 offset:3072\n\tv_fma_f32 v181, -v117, v181, v117\n\tv_nop\n\tv_exp_f32 v185, v121\n\tv_add_f32 v214, v214, v214\n\tds_read_b64_tr_b16 v[60:61], v13 offset:16384\n\tv_fract_f32 v218, v212\n\tds_read_b128 a[120:123], v28 offset:3072\n\tv_fma_f32 v184, v184, s69, s69\n\tv_mfma_f32_32x32x16_bf16 v[124:139], v[80:83], v[232:235], v[124:139]\n\tds_read_b64_tr_b16 v[62:63], v13 offset:53248\n\tds_read_b64_tr_b16 v[64:65], v13 offset:17408\n\tds_read_b64_tr_b16 v[66:67], v13 offset:54272\n\tv_rcp_f32 v183, v183\n\tv_add_f32 v219, v213, v212\n\tv_fract_f32 v219, v219\n\tv_fma_f32 v184, v184, v184, 0.5\n\tv_cos_f32 v215, v218\n\ts_add_u32 m0, s64, 9216\n\tds_read_b128 a[128:131], v25 offset:4096\n\tv_fma_f32 v182, -v118, v182, v118\n\tv_cvt_pk_bf16_f32 v192, v180, v181\n\ts_add_u32 s98, s98, 0xd800\n\ts_addc_u32 s99, s99, 0\n\tv_cos_f32 v216, v219\n\tv_exp_f32 v186, v122\n\tbuffer_load_dwordx4 v[204:207], v196, s[56:59], s79 offen sc1\n\ts_add_u32 s79, s79, 4096\n\tds_read_b128 a[136:139], v26 offset:4096\n\tv_fma_f32 v185, v185, s69, s69\n\tv_sin_f32 v218, v218\n\tv_rcp_f32 v184, v184\n\tv_mfma_f32_32x32x16_bf16 v[124:139], v[84:87], v[240:243], v[124:139]\n\tglobal_load_lds_dwordx4 v5, s[98:99] offset:0\n\tds_read_b128 a[144:147], v27 offset:4096\n\tv_fma_f32 v185, v185, v185, 0.5\n\tv_sin_f32 v219, v219\n\tv_fma_f32 v183, -v119, v183, v119\n\tv_exp_f32 v187, v123\n\tv_cvt_pk_bf16_f32 v140, v215, v216\n\tds_read_b128 a[152:155], v28 offset:4096\n\tv_cvt_pk_bf16_f32 v148, v218, v219\n\tv_fma_f32 v186, v186, s69, s69\n\tv_fma_f32 v217, v214, v216, -v215\n\tv_rcp_f32 v185, v185\n\tglobal_load_lds_dwordx4 v6, s[98:99] offset:1024\n\tv_fma_f32 v37, v214, v219, -v218\n\tds_read_b128 a[160:163], v25 offset:5120\n\tv_fma_f32 v186, v186, v186, 0.5\n\tv_fma_f32 v215, v214, v217, -v216\n\tv_fma_f32 v184, -v120, v184, v120\n\tbuffer_load_dwordx4 v[208:211], v196, s[56:59], s79 offen sc1\n\ts_add_u32 s79, s79, 4096\n\tv_mfma_f32_32x32x16_bf16 v[124:139], v[88:91], v[248:251], v[124:139]\n\tv_fma_f32 v218, v214, v37, -v219\n\tv_cvt_pk_bf16_f32 v193, v182, v183\n\tv_cvt_pk_bf16_f32 v141, v217, v215\n\tv_fma_f32 v187, v187, s69, s69\n\tds_read_b128 a[168:171], v26 offset:5120\n\tv_rcp_f32 v186, v186\n\tv_cvt_pk_bf16_f32 v149, v37, v218\n\tv_fma_f32 v216, v214, v215, -v217\n\tv_fma_f32 v187, v187, v187, 0.5\n\tglobal_load_lds_dwordx4 v7, s[98:99] offset:2048\n\tv_fma_f32 v219, v214, v218, -v37\n\tds_read_b128 a[176:179], v27 offset:5120\n\tv_fma_f32 v185, -v121, v185, v121\n\tv_fma_f32 v217, v214, v216, -v215\n\tv_rcp_f32 v187, v187\n\tv_fma_f32 v37, v214, v219, -v218\n\tv_fma_f32 v186, -v122, v186, v122\n\tv_cvt_pk_bf16_f32 v142, v216, v217\n\tds_read_b128 a[184:187], v28 offset:5120\n\tv_cvt_pk_bf16_f32 v194, v184, v185\n\tv_cvt_pk_bf16_f32 v150, v219, v37\n\tv_fma_f32 v187, -v123, v187, v123\n\tv_fma_f32 v215, v214, v217, -v216\n\tv_cvt_pk_bf16_f32 v195, v186, v187\n\tds_write_b128 v21, v[188:191] offset:6144\n\tds_write_b128 v22, v[192:195] offset:6144\n\ts_waitcnt vmcnt(9) lgkmcnt(0)\n\ts_barrier\n\tv_mfma_f32_32x32x16_bf16 v[92:107], v[44:47], a[0:3], 0\n\tds_read_b64_tr_b16 v[68:69], v13 offset:0\n\ts_add_u32 m0, s64, 12288\n\tds_read_b128 a[192:195], v25 offset:6144\n\tv_fma_f32 v218, v214, v37, -v219\n\tv_fma_f32 v216, v214, v215, -v217\n\tds_read_b64_tr_b16 v[70:71], v13 offset:36864\n\ts_add_u32 s98, s98, 0xd800\n\ts_addc_u32 s99, s99, 0\n\tv_fma_f32 v219, v214, v218, -v37\n\tds_read_b128 a[196:199], v29 offset:6144\n\tv_cvt_pk_bf16_f32 v143, v215, v216\n\tv_mfma_f32_32x32x16_bf16 v[108:123], v[44:47], a[4:7], 0\n\tv_exp_f32 v172, v124\n\tds_read_b64_tr_b16 v[72:73], v13 offset:1024\n\tv_cvt_pk_bf16_f32 v151, v218, v219\n\tv_exp_f32 v173, v125\n\tglobal_load_lds_dwordx4 v5, s[98:99] offset:0\n\tds_read_b128 a[200:203], v26 offset:6144\n\ts_mov_b32 s90, 0x42000000\n\tv_fma_f32 v218, v213, s90, v212\n\tv_fma_f32 v172, v172, s69, s69\n\tv_fma_f32 v172, v172, v172, 0.5\n\tv_mfma_f32_32x32x16_bf16 v[92:107], v[48:51], a[8:11], v[92:107]\n\tds_read_b64_tr_b16 v[74:75], v13 offset:37888\n\tv_exp_f32 v174, v126\n\tv_fract_f32 v218, v218\n\tds_read_b128 a[204:207], v30 offset:6144\n\tv_fma_f32 v173, v173, s69, s69\n\ts_mov_b32 s90, 0x42040000\n\tv_fma_f32 v219, v213, s90, v212\n\tds_read_b64_tr_b16 v[76:77], v13 offset:2048\n\tv_rcp_f32 v172, v172\n\tglobal_load_lds_dwordx4 v6, s[98:99] offset:1024\n\tv_mfma_f32_32x32x16_bf16 v[108:123], v[48:51], a[12:15], v[108:123]\n\tv_fma_f32 v173, v173, v173, 0.5\n\tds_read_b64_tr_b16 v[78:79], v13 offset:38912\n\tds_read_b128 a[208:211], v27 offset:6144\n\tv_exp_f32 v175, v127\n\tv_fract_f32 v219, v219\n\tv_cos_f32 v215, v218\n\tv_fma_f32 v174, v174, s69, s69\n\tv_rcp_f32 v173, v173\n\tds_read_b128 a[212:215], v31 offset:6144\n\tv_mfma_f32_32x32x16_bf16 v[92:107], v[52:55], a[16:19], v[92:107]\n\tds_read_b64_tr_b16 v[80:81], v13 offset:3072\n\tv_fma_f32 v174, v174, v174, 0.5\n\tv_cos_f32 v216, v219\n\tv_fma_f32 v172, -v124, v172, v124\n\tds_read_b128 a[216:219], v28 offset:6144\n\tv_exp_f32 v176, v128\n\tglobal_load_lds_dwordx4 v7, s[98:99] offset:2048\n\tds_read_b64_tr_b16 v[82:83], v13 offset:39936\n\tv_mfma_f32_32x32x16_bf16 v[108:123], v[52:55], a[20:23], v[108:123]\n\tv_fma_f32 v175, v175, s69, s69\n\tv_sin_f32 v218, v218\n\tv_rcp_f32 v174, v174\n\tds_read_b64_tr_b16 v[84:85], v13 offset:4096\n\tv_fma_f32 v175, v175, v175, 0.5\n\tv_sin_f32 v219, v219\n\tds_read_b128 a[220:223], v32 offset:6144\n\tv_fma_f32 v173, -v125, v173, v125\n\tv_exp_f32 v177, v129\n\tv_cvt_pk_bf16_f32 v144, v215, v216\n\tv_mfma_f32_32x32x16_bf16 v[92:107], v[56:59], a[24:27], v[92:107]\n\tds_read_b64_tr_b16 v[86:87], v13 offset:40960\n\tv_fma_f32 v176, v176, s69, s69\n\tv_rcp_f32 v175, v175\n\tds_read_b128 a[224:227], v25 offset:7168\n\ts_add_u32 m0, s64, 15360\n\tv_cvt_pk_bf16_f32 v152, v218, v219\n\tv_fma_f32 v176, v176, v176, 0.5\n\ts_add_u32 s98, s98, 0xd800\n\ts_addc_u32 s99, s99, 0\n\tv_fma_f32 v217, v214, v216, -v215\n\tds_read_b64_tr_b16 v[88:89], v13 offset:5120\n\tv_fma_f32 v174, -v126, v174, v126\n\tv_mfma_f32_32x32x16_bf16 v[108:123], v[56:59], a[28:31], v[108:123]\n\tds_read_b64_tr_b16 v[90:91], v13 offset:41984\n\tv_cvt_pk_bf16_f32 v188, v172, v173\n\tds_read_b128 a[228:231], v29 offset:7168\n\tv_fma_f32 v37, v214, v219, -v218\n\tv_exp_f32 v178, v130\n\tv_fma_f32 v215, v214, v217, -v216\n\tglobal_load_lds_dwordx4 v5, s[98:99] offset:0\n\tv_fma_f32 v177, v177, s69, s69\n\tv_fma_f32 v218, v214, v37, -v219\n\tv_mfma_f32_32x32x16_bf16 v[92:107], v[60:63], a[32:35], v[92:107]\n\tv_rcp_f32 v176, v176\n\tds_read_b128 a[232:235], v26 offset:7168\n\tv_cvt_pk_bf16_f32 v145, v217, v215\n\tv_fma_f32 v177, v177, v177, 0.5\n\tv_fma_f32 v175, -v127, v175, v127\n\tv_cvt_pk_bf16_f32 v153, v37, v218\n\tv_exp_f32 v179, v131\n\tds_read_b128 a[236:239], v30 offset:7168\n\tv_fma_f32 v216, v214, v215, -v217\n\tv_fma_f32 v178, v178, s69, s69\n\tv_fma_f32 v219, v214, v218, -v37\n\tv_rcp_f32 v177, v177\n\tv_mfma_f32_32x32x16_bf16 v[108:123], v[60:63], a[36:39], v[108:123]\n\tglobal_load_lds_dwordx4 v6, s[98:99] offset:1024\n\tv_fma_f32 v217, v214, v216, -v215\n\tv_fma_f32 v178, v178, v178, 0.5\n\tds_read_b128 a[240:243], v27 offset:7168\n\tv_fma_f32 v176, -v128, v176, v128\n\tv_fma_f32 v37, v214, v219, -v218\n\tv_cvt_pk_bf16_f32 v189, v174, v175\n\tv_exp_f32 v180, v132\n\tv_mfma_f32_32x32x16_bf16 v[92:107], v[64:67], a[40:43], v[92:107]\n\tv_cvt_pk_bf16_f32 v146, v216, v217\n\tds_read_b128 a[244:247], v31 offset:7168\n\tv_fma_f32 v179, v179, s69, s69\n\tv_cvt_pk_bf16_f32 v154, v219, v37\n\tv_rcp_f32 v178, v178\n\tv_fma_f32 v215, v214, v217, -v216\n\tv_fma_f32 v179, v179, v179, 0.5\n\tglobal_load_lds_dwordx4 v7, s[98:99] offset:2048\n\tv_fma_f32 v218, v214, v37, -v219\n\tv_mfma_f32_32x32x16_bf16 v[108:123], v[64:67], a[44:47], v[108:123]\n\tv_fma_f32 v177, -v129, v177, v129\n\tds_read_b128 a[248:251], v28 offset:7168\n\tv_exp_f32 v181, v133\n\tv_fma_f32 v216, v214, v215, -v217\n\tv_fma_f32 v180, v180, s69, s69\n\tv_fma_f32 v219, v214, v218, -v37\n\tv_rcp_f32 v179, v179\n\tds_read_b128 a[252:255], v32 offset:7168\n\tv_cvt_pk_bf16_f32 v147, v215, v216\n\tv_fma_f32 v180, v180, v180, 0.5\n\tv_cvt_pk_bf16_f32 v155, v218, v219\n\tv_fma_f32 v178, -v130, v178, v130\n\ts_waitcnt vmcnt(6) lgkmcnt(7)\n\ts_barrier\n\tv_mfma_f32_32x32x16_bf16 v[92:107], v[68:71], a[48:51], v[92:107]\n\tds_read_b64_tr_b16 v[44:45], v13 offset:6144\n\ts_add_u32 m0, s64, 0\n\tv_cvt_pk_bf16_f32 v190, v176, v177\n\tv_exp_f32 v182, v134\n\tds_read_b64_tr_b16 v[46:47], v13 offset:43008\n\ts_add_u32 s98, s98, 0xd800\n\ts_addc_u32 s99, s99, 0\n\tv_fma_f32 v181, v181, s69, s69\n\tv_rcp_f32 v180, v180\n\tv_mfma_f32_32x32x16_bf16 v[108:123], v[68:71], a[52:55], v[108:123]\n\tds_read_b64_tr_b16 v[48:49], v13 offset:7168\n\tglobal_load_lds_dwordx4 v5, s[98:99] offset:0\n\tv_fma_f32 v181, v181, v181, 0.5\n\tv_fma_f32 v179, -v131, v179, v131\n\tv_exp_f32 v183, v135\n\tv_mfma_f32_32x32x16_bf16 v[92:107], v[72:75], a[56:59], v[92:107]\n\tds_read_b64_tr_b16 v[50:51], v13 offset:44032\n\tv_fma_f32 v182, v182, s69, s69\n\tv_rcp_f32 v181, v181\n\tds_read_b64_tr_b16 v[52:53], v13 offset:8192\n\tv_fma_f32 v182, v182, v182, 0.5\n\tds_write_b128 v197, v[200:203] offset:0\n\tv_mfma_f32_32x32x16_bf16 v[108:123], v[72:75], a[60:63], v[108:123]\n\tglobal_load_lds_dwordx4 v6, s[98:99] offset:1024\n\tv_fma_f32 v180, -v132, v180, v132\n\tds_read_b64_tr_b16 v[54:55], v13 offset:45056\n\tv_cvt_pk_bf16_f32 v191, v178, v179\n\tv_mfma_f32_32x32x16_bf16 v[92:107], v[76:79], a[64:67], v[92:107]\n\tv_exp_f32 v184, v136\n\tds_read_b64_tr_b16 v[56:57], v13 offset:9216\n\tv_fma_f32 v183, v183, s69, s69\n\tv_rcp_f32 v182, v182\n\tglobal_load_lds_dwordx4 v7, s[98:99] offset:2048\n\tv_mfma_f32_32x32x16_bf16 v[108:123], v[76:79], a[68:71], v[108:123]\n\tds_read_b64_tr_b16 v[58:59], v13 offset:46080\n\tv_fma_f32 v183, v183, v183, 0.5\n\tv_fma_f32 v181, -v133, v181, v133\n\tv_exp_f32 v185, v137\n\tds_read_b64_tr_b16 v[60:61], v13 offset:10240\n\tds_write_b128 v197, v[204:207] offset:4096\n\tv_mfma_f32_32x32x16_bf16 v[92:107], v[80:83], a[72:75], v[92:107]\n\tv_fma_f32 v184, v184, s69, s69\n\tv_rcp_f32 v183, v183\n\tds_read_b64_tr_b16 v[62:63], v13 offset:47104\n\tv_fma_f32 v184, v184, v184, 0.5\n\ts_add_u32 m0, s64, 3072\n\tv_mfma_f32_32x32x16_bf16 v[108:123], v[80:83], a[76:79], v[108:123]\n\tds_read_b64_tr_b16 v[64:65], v13 offset:11264\n\tds_read_b64_tr_b16 v[66:67], v13 offset:48128\n\tv_fma_f32 v182, -v134, v182, v134\n\tv_cvt_pk_bf16_f32 v192, v180, v181\n\ts_add_u32 s98, s98, 0xd800\n\ts_addc_u32 s99, s99, 0\n\tv_exp_f32 v186, v138\n\tv_fma_f32 v185, v185, s69, s69\n\tv_rcp_f32 v184, v184\n\tv_mfma_f32_32x32x16_bf16 v[92:107], v[84:87], a[80:83], v[92:107]\n\tglobal_load_lds_dwordx4 v5, s[98:99] offset:0\n\tv_fma_f32 v185, v185, v185, 0.5\n\tv_fma_f32 v183, -v135, v183, v135\n\tv_exp_f32 v187, v139\n\tv_fma_f32 v186, v186, s69, s69\n\tds_write_b128 v197, v[208:211] offset:8192\n\tv_mfma_f32_32x32x16_bf16 v[108:123], v[84:87], a[84:87], v[108:123]\n\tv_rcp_f32 v185, v185\n\tglobal_load_lds_dwordx4 v6, s[98:99] offset:1024\n\tv_mfma_f32_32x32x16_bf16 v[92:107], v[88:91], a[88:91], v[92:107]\n\tv_fma_f32 v186, v186, v186, 0.5\n\tv_fma_f32 v184, -v136, v184, v136\n\tv_cvt_pk_bf16_f32 v193, v182, v183\n\tv_fma_f32 v187, v187, s69, s69\n\tv_rcp_f32 v186, v186\n\tv_fma_f32 v187, v187, v187, 0.5\n\tv_mfma_f32_32x32x16_bf16 v[108:123], v[88:91], a[92:95], v[108:123]\n\tglobal_load_lds_dwordx4 v7, s[98:99] offset:2048\n\tv_fma_f32 v185, -v137, v185, v137\n\tv_rcp_f32 v187, v187\n\tv_fma_f32 v186, -v138, v186, v138\n\tv_cvt_pk_bf16_f32 v194, v184, v185\n\tv_fma_f32 v187, -v139, v187, v139\n\tv_cvt_pk_bf16_f32 v195, v186, v187\n\tds_write_b128 v23, v[188:191]\n\tds_write_b128 v24, v[192:195]\n\ts_waitcnt vmcnt(6) lgkmcnt(0)\n\ts_barrier\n\tv_mfma_f32_32x32x16_bf16 v[92:107], v[44:47], a[96:99], v[92:107]\n\tds_read_b64_tr_b16 v[68:69], v13 offset:12288\n\ts_add_u32 m0, s64, 6144\n\tds_read_b128 v[224:227], v25 offset:8192\n\ts_add_u32 s98, s98, 0xd800\n\ts_addc_u32 s99, s99, 0\n\tds_read_b64_tr_b16 v[70:71], v13 offset:49152\n\tv_bfe_u32 v220, v0, 2, 2\n\tv_lshrrev_b32 v222, 4, v0\n\tv_lshl_add_u32 v220, v222, 3, v220\n\tv_mul_u32_u24 v220, 160, v220\n\tv_and_b32 v222, 3, v0\n\tv_lshl_add_u32 v220, v222, 3, v220\n\tv_add_u32 v220, 150016, v220\n\tv_mfma_f32_32x32x16_bf16 v[108:123], v[44:47], a[100:103], v[108:123]\n\tds_read_b64_tr_b16 v[72:73], v13 offset:13312\n\tglobal_load_lds_dwordx4 v5, s[98:99] offset:0\n\tds_read_b64_tr_b16 v[196:197], v220 offset:0\n\tv_mfma_f32_32x32x16_bf16 v[92:107], v[48:51], a[104:107], v[92:107]\n\tds_read_b64_tr_b16 v[198:199], v220 offset:640\n\tds_read_b64_tr_b16 v[74:75], v13 offset:50176\n\tds_read_b128 v[228:231], v29 offset:8192\n\tds_read_b64_tr_b16 v[200:201], v220 offset:5120\n\tds_read_b64_tr_b16 v[202:203], v220 offset:5760\n\tds_read_b64_tr_b16 v[204:205], v220 offset:32\n\tds_read_b64_tr_b16 v[206:207], v220 offset:672\n\tds_read_b64_tr_b16 v[208:209], v220 offset:5152\n\tds_read_b64_tr_b16 v[76:77], v13 offset:14336\n\tv_mfma_f32_32x32x16_bf16 v[108:123], v[48:51], a[108:111], v[108:123]\n\tds_read_b64_tr_b16 v[210:211], v220 offset:5792\n\ts_waitcnt lgkmcnt(9)\n\tv_mfma_f32_16x16x32_bf16 v[212:215], v[196:199], v[140:143], 0\n\tds_read_b64_tr_b16 v[78:79], v13 offset:51200\n\tglobal_load_lds_dwordx4 v6, s[98:99] offset:1024\n\tv_mfma_f32_32x32x16_bf16 v[92:107], v[52:55], a[112:115], v[92:107]\n\tds_read_b128 v[232:235], v26 offset:8192\n\tds_read_b64_tr_b16 v[80:81], v13 offset:15360\n\tv_mfma_f32_16x16x32_bf16 v[216:219], v[196:199], v[148:151], 0\n\ts_waitcnt lgkmcnt(8)\n\tv_mfma_f32_16x16x32_bf16 v[212:215], v[200:203], v[144:147], v[212:215]\n\tv_mfma_f32_32x32x16_bf16 v[108:123], v[52:55], a[116:119], v[108:123]\n\tds_read_b64_tr_b16 v[82:83], v13 offset:52224\n\tglobal_load_lds_dwordx4 v7, s[98:99] offset:2048\n\tds_read_b128 v[236:239], v30 offset:8192\n\tv_mfma_f32_16x16x32_bf16 v[216:219], v[200:203], v[152:155], v[216:219]\n\tv_mfma_f32_32x32x16_bf16 v[92:107], v[56:59], a[120:123], v[92:107]\n\tds_read_b64_tr_b16 v[84:85], v13 offset:16384\n\tds_read_b64_tr_b16 v[86:87], v13 offset:53248\n\tds_read_b64_tr_b16 v[196:197], v220 offset:64\n\tds_read_b64_tr_b16 v[198:199], v220 offset:704\n\tds_read_b128 v[240:243], v27 offset:8192\n\ts_add_u32 m0, s64, 9216\n\tds_read_b64_tr_b16 v[200:201], v220 offset:5184\n\tds_read_b64_tr_b16 v[202:203], v220 offset:5824\n\ts_add_u32 s98, s98, 0xd800\n\ts_addc_u32 s99, s99, 0\n\tv_mfma_f32_32x32x16_bf16 v[108:123], v[56:59], a[124:127], v[108:123]\n\tds_read_b64_tr_b16 v[88:89], v13 offset:17408\n\tds_read_b64_tr_b16 v[90:91], v13 offset:54272\n\tv_sub_f32 v212, v212, v217\n\tv_add_f32 v213, v213, v216\n\tv_sub_f32 v214, v214, v219\n\tv_add_f32 v215, v215, v218\n\tv_cvt_pk_bf16_f32 v156, v212, v213\n\tglobal_load_lds_dwordx4 v5, s[98:99] offset:0\n\tv_mfma_f32_32x32x16_bf16 v[92:107], v[60:63], a[128:131], v[92:107]\n\tv_cvt_pk_bf16_f32 v157, v214, v215\n\tds_read_b128 v[244:247], v31 offset:8192\n\ts_waitcnt lgkmcnt(15)\n\tv_mfma_f32_16x16x32_bf16 v[212:215], v[204:207], v[140:143], 0\n\tv_mfma_f32_16x16x32_bf16 v[216:219], v[204:207], v[148:151], 0\n\tv_mfma_f32_32x32x16_bf16 v[108:123], v[60:63], a[132:135], v[108:123]\n\tglobal_load_lds_dwordx4 v6, s[98:99] offset:1024\n\tds_read_b128 v[248:251], v28 offset:8192\n\tv_mfma_f32_16x16x32_bf16 v[212:215], v[208:211], v[144:147], v[212:215]\n\tv_mfma_f32_32x32x16_bf16 v[92:107], v[64:67], a[136:139], v[92:107]\n\tv_mfma_f32_16x16x32_bf16 v[216:219], v[208:211], v[152:155], v[216:219]\n\tds_read_b128 v[252:255], v32 offset:8192\n\tds_read_b64_tr_b16 v[204:205], v220 offset:96\n\tglobal_load_lds_dwordx4 v7, s[98:99] offset:2048\n\tv_mfma_f32_32x32x16_bf16 v[108:123], v[64:67], a[140:143], v[108:123]\n\tds_read_b64_tr_b16 v[206:207], v220 offset:736\n\tds_read_b64_tr_b16 v[208:209], v220 offset:5216\n\tds_read_b64_tr_b16 v[210:211], v220 offset:5856\n\ts_nop 0\n\tv_sub_f32 v212, v212, v217\n\tv_add_f32 v213, v213, v216\n\tv_sub_f32 v214, v214, v219\n\tv_add_f32 v215, v215, v218\n\ts_waitcnt vmcnt(6) lgkmcnt(7)\n\ts_barrier\n\tv_mfma_f32_32x32x16_bf16 v[92:107], v[68:71], a[144:147], v[92:107]\n\tds_read_b64_tr_b16 v[44:45], v13 offset:0\n\ts_add_u32 m0, s64, 12288\n\tv_cvt_pk_bf16_f32 v158, v212, v213\n\tv_cvt_pk_bf16_f32 v159, v214, v215\n\tds_read_b64_tr_b16 v[46:47], v13 offset:36864\n\tv_mfma_f32_16x16x32_bf16 v[212:215], v[196:199], v[140:143], 0\n\tv_mfma_f32_32x32x16_bf16 v[108:123], v[68:71], a[148:151], v[108:123]\n\ts_add_u32 s98, s86, 0x0\n\ts_add_u32 s98, s98, s24\n\ts_addc_u32 s99, s25, 0\n\tds_read_b64_tr_b16 v[48:49], v13 offset:1024\n\tv_mfma_f32_16x16x32_bf16 v[216:219], v[196:199], v[148:151], 0\n\tv_mfma_f32_16x16x32_bf16 v[212:215], v[200:203], v[144:147], v[212:215]\n\tv_mfma_f32_32x32x16_bf16 v[92:107], v[72:75], a[152:155], v[92:107]\n\tds_read_b64_tr_b16 v[50:51], v13 offset:37888\n\tglobal_load_lds_dwordx4 v5, s[98:99] offset:0\n\tds_read_b64_tr_b16 v[52:53], v13 offset:2048\n\tv_mfma_f32_32x32x16_bf16 v[108:123], v[72:75], a[156:159], v[108:123]\n\tv_mfma_f32_16x16x32_bf16 v[216:219], v[200:203], v[152:155], v[216:219]\n\tds_read_b64_tr_b16 v[54:55], v13 offset:38912\n\tds_read_b64_tr_b16 v[196:197], v220 offset:128\n\tds_read_b64_tr_b16 v[198:199], v220 offset:768\n\tds_read_b64_tr_b16 v[200:201], v220 offset:5248\n\tds_read_b64_tr_b16 v[202:203], v220 offset:5888\n\tglobal_load_lds_dwordx4 v6, s[98:99] offset:1024\n\tv_mfma_f32_32x32x16_bf16 v[92:107], v[76:79], a[160:163], v[92:107]\n\tds_read_b64_tr_b16 v[56:57], v13 offset:3072\n\tv_sub_f32 v212, v212, v217\n\tv_add_f32 v213, v213, v216\n\tv_sub_f32 v214, v214, v219\n\tv_add_f32 v215, v215, v218\n\tv_cvt_pk_bf16_f32 v160, v212, v213\n\tv_mfma_f32_32x32x16_bf16 v[108:123], v[76:79], a[164:167], v[108:123]\n\tds_read_b64_tr_b16 v[58:59], v13 offset:39936\n\tv_cvt_pk_bf16_f32 v161, v214, v215\n\ts_waitcnt lgkmcnt(14)\n\tv_mfma_f32_16x16x32_bf16 v[212:215], v[204:207], v[140:143], 0\n\tds_read_b64_tr_b16 v[60:61], v13 offset:4096\n\tglobal_load_lds_dwordx4 v7, s[98:99] offset:2048\n\tv_mfma_f32_32x32x16_bf16 v[92:107], v[80:83], a[168:171], v[92:107]\n\tv_mfma_f32_16x16x32_bf16 v[216:219], v[204:207], v[148:151], 0\n\tds_read_b64_tr_b16 v[62:63], v13 offset:40960\n\tds_read_b64_tr_b16 v[64:65], v13 offset:5120\n\ts_waitcnt lgkmcnt(15)\n\tv_mfma_f32_16x16x32_bf16 v[212:215], v[208:211], v[144:147], v[212:215]\n\tv_mfma_f32_32x32x16_bf16 v[108:123], v[80:83], a[172:175], v[108:123]\n\tds_read_b64_tr_b16 v[66:67], v13 offset:41984\n\tv_mfma_f32_16x16x32_bf16 v[216:219], v[208:211], v[152:155], v[216:219]\n\ts_add_u32 m0, s64, 15360\n\ts_nop 6\n\tv_sub_f32 v212, v212, v217\n\tv_mfma_f32_32x32x16_bf16 v[92:107], v[84:87], a[176:179], v[92:107]\n\tv_add_f32 v213, v213, v216\n\ts_add_u32 s98, s98, 0xd800\n\ts_addc_u32 s99, s99, 0\n\tv_sub_f32 v214, v214, v219\n\tv_add_f32 v215, v215, v218\n\tv_cvt_pk_bf16_f32 v162, v212, v213\n\tv_cvt_pk_bf16_f32 v163, v214, v215\n\ts_waitcnt lgkmcnt(8)\n\tv_mfma_f32_16x16x32_bf16 v[212:215], v[196:199], v[140:143], 0\n\tv_mfma_f32_32x32x16_bf16 v[108:123], v[84:87], a[180:183], v[108:123]\n\tglobal_load_lds_dwordx4 v5, s[98:99] offset:0\n\tv_mfma_f32_16x16x32_bf16 v[216:219], v[196:199], v[148:151], 0\n\tv_mfma_f32_32x32x16_bf16 v[92:107], v[88:91], a[184:187], v[92:107]\n\ts_waitcnt lgkmcnt(6)\n\tv_mfma_f32_16x16x32_bf16 v[212:215], v[200:203], v[144:147], v[212:215]\n\tglobal_load_lds_dwordx4 v6, s[98:99] offset:1024\n\tv_mfma_f32_32x32x16_bf16 v[108:123], v[88:91], a[188:191], v[108:123]\n\tv_mfma_f32_16x16x32_bf16 v[216:219], v[200:203], v[152:155], v[216:219]\n\ts_nop 7\n\tv_sub_f32 v212, v212, v217\n\tv_add_f32 v213, v213, v216\n\tv_sub_f32 v214, v214, v219\n\tv_add_f32 v215, v215, v218\n\tv_cvt_pk_bf16_f32 v164, v212, v213\n\tv_cvt_pk_bf16_f32 v165, v214, v215\n\ts_waitcnt vmcnt(5) lgkmcnt(0)\n\ts_barrier\n\tv_mfma_f32_32x32x16_bf16 v[92:107], v[44:47], a[192:195], v[92:107]\n\tds_read_b64_tr_b16 v[68:69], v13 offset:6144\n\ts_add_u32 m0, s64, 0\n\ts_add_u32 s98, s81, 0x100\n\ts_add_u32 s98, s98, s48\n\ts_addc_u32 s99, s49, 0\n\tds_read_b64_tr_b16 v[70:71], v13 offset:43008\n\tv_mfma_f32_32x32x16_bf16 v[108:123], v[44:47], a[196:199], v[108:123]\n\tds_read_b64_tr_b16 v[72:73], v13 offset:7168\n\tglobal_load_lds_dwordx4 v5, s[98:99] offset:0\n\tv_and_b32 v220, 15, v0\n\ts_mul_i32 s95, s62, 16\n\tv_add_u32 v220, s95, v220\n\tv_mul_u32_u24 v220, 176, v220\n\tv_lshrrev_b32 v222, 4, v0\n\tv_lshl_add_u32 v220, v222, 3, v220\n\tv_add_u32 v220, 150016, v220\n\tv_mfma_f32_32x32x16_bf16 v[92:107], v[48:51], a[200:203], v[92:107]\n\tds_read_b64_tr_b16 v[74:75], v13 offset:44032\n\tds_read_b64_tr_b16 v[76:77], v13 offset:8192\n\tv_mfma_f32_32x32x16_bf16 v[108:123], v[48:51], a[204:207], v[108:123]\n\tds_write_b64 v220, v[156:157] offset:0\n\tv_mfma_f32_32x32x16_bf16 v[92:107], v[52:55], a[208:211], v[92:107]\n\tds_read_b64_tr_b16 v[78:79], v13 offset:45056\n\tglobal_load_lds_dwordx4 v6, s[98:99] offset:1024\n\tds_read_b64_tr_b16 v[80:81], v13 offset:9216\n\tds_read_b64_tr_b16 v[82:83], v13 offset:46080\n\tv_mfma_f32_32x32x16_bf16 v[108:123], v[52:55], a[212:215], v[108:123]\n\tglobal_load_lds_dwordx4 v7, s[98:99] offset:2048\n\tds_read_b64_tr_b16 v[84:85], v13 offset:10240\n\tds_write_b64 v220, v[158:159] offset:32\n\tv_mfma_f32_32x32x16_bf16 v[92:107], v[56:59], a[216:219], v[92:107]\n\tds_read_b64_tr_b16 v[86:87], v13 offset:47104\n\ts_add_u32 m0, s64, 3072\n\tds_read_b64_tr_b16 v[88:89], v13 offset:11264\n\ts_add_u32 s98, s98, 0xd800\n\ts_addc_u32 s99, s99, 0\n\tv_mfma_f32_32x32x16_bf16 v[108:123], v[56:59], a[220:223], v[108:123]\n\tds_read_b64_tr_b16 v[90:91], v13 offset:48128\n\tglobal_load_lds_dwordx4 v5, s[98:99] offset:0\n\tv_mfma_f32_32x32x16_bf16 v[92:107], v[60:63], a[224:227], v[92:107]\n\tds_write_b64 v220, v[160:161] offset:64\n\tv_mfma_f32_32x32x16_bf16 v[108:123], v[60:63], a[228:231], v[108:123]\n\tds_write_b64 v220, v[162:163] offset:96\n\tv_mfma_f32_32x32x16_bf16 v[92:107], v[64:67], a[232:235], v[92:107]\n\tglobal_load_lds_dwordx4 v6, s[98:99] offset:1024\n\tds_write_b64 v220, v[164:165] offset:128\n\tv_mfma_f32_32x32x16_bf16 v[108:123], v[64:67], a[236:239], v[108:123]\n\tglobal_load_lds_dwordx4 v7, s[98:99] offset:2048\n\ts_waitcnt vmcnt(6) lgkmcnt(0)\n\ts_barrier\n\tv_mfma_f32_32x32x16_bf16 v[92:107], v[68:71], a[240:243], v[92:107]\n\tds_read_b64_tr_b16 v[44:45], v13 offset:12288\n\ts_add_u32 m0, s64, 6144\n\ts_add_u32 s98, s98, 0xd800\n\ts_addc_u32 s99, s99, 0\n\tds_read_b64_tr_b16 v[46:47], v13 offset:49152\n\tv_and_b32 v220, 31, v0\n\ts_and_b32 s93, s62, 1\n\ts_xor_b32 s94, s93, 0\n\ts_mul_i32 s95, s94, 32\n\tv_add_u32 v220, s95, v220\n\tv_mul_u32_u24 v220, 176, v220\n\tv_lshrrev_b32 v223, 5, v0\n\tv_lshl_add_u32 v220, v223, 4, v220\n\tv_add_u32 v220, 150016, v220\n\tv_mfma_f32_32x32x16_bf16 v[108:123], v[68:71], a[244:247], v[108:123]\n\tglobal_load_lds_dwordx4 v5, s[98:99] offset:0\n\tds_read_b64_tr_b16 v[48:49], v13 offset:13312\n\tv_mfma_f32_32x32x16_bf16 v[92:107], v[72:75], a[248:251], v[92:107]\n\tglobal_load_lds_dwordx4 v6, s[98:99] offset:1024\n\tds_read_b64_tr_b16 v[50:51], v13 offset:50176\n\tds_read_b64_tr_b16 v[52:53], v13 offset:14336\n\tglobal_load_lds_dwordx4 v7, s[98:99] offset:2048\n\tv_mfma_f32_32x32x16_bf16 v[108:123], v[72:75], a[252:255], v[108:123]\n\ts_add_u32 m0, s64, 9216\n\ts_add_u32 s98, s98, 0xd800\n\ts_addc_u32 s99, s99, 0\n\tds_read_b128 v[156:159], v220 offset:0\n\tds_read_b128 v[164:167], v220 offset:32\n\tds_read_b64_tr_b16 v[54:55], v13 offset:51200\n\tglobal_load_lds_dwordx4 v5, s[98:99] offset:0\n\tv_mfma_f32_32x32x16_bf16 v[92:107], v[76:79], v[224:227], v[92:107]\n\tds_read_b128 v[172:175], v220 offset:64\n\tglobal_load_lds_dwordx4 v6, s[98:99] offset:1024\n\tds_read_b128 v[180:183], v220 offset:96\n\tds_read_b64_tr_b16 v[56:57], v13 offset:15360\n\tds_read_b128 v[196:199], v220 offset:128\n\tglobal_load_lds_dwordx4 v7, s[98:99] offset:2048\n\tv_mfma_f32_32x32x16_bf16 v[108:123], v[76:79], v[228:231], v[108:123]\n\tds_read_b64_tr_b16 v[58:59], v13 offset:52224\n\ts_add_u32 m0, s64, 73728\n\ts_add_u32 s98, s98, 0x28800\n\ts_addc_u32 s99, s99, 0\n\tds_read_b64_tr_b16 v[60:61], v13 offset:16384\n\tv_and_b32 v221, 31, v0\n\ts_and_b32 s93, s62, 1\n\ts_xor_b32 s94, s93, 1\n\ts_mul_i32 s95, s94, 32\n\tv_add_u32 v221, s95, v221\n\tv_mul_u32_u24 v221, 176, v221\n\tv_lshrrev_b32 v223, 5, v0\n\tv_lshl_add_u32 v221, v223, 4, v221\n\tv_add_u32 v221, 150016, v221\n\tv_mfma_f32_32x32x16_bf16 v[92:107], v[80:83], v[232:235], v[92:107]\n\tds_read_b64_tr_b16 v[62:63], v13 offset:53248\n\tglobal_load_lds_dwordx4 v5, s[98:99] offset:0\n\tv_mfma_f32_32x32x16_bf16 v[108:123], v[80:83], v[236:239], v[108:123]\n\tglobal_load_lds_dwordx4 v6, s[98:99] offset:1024\n\tds_read_b128 v[160:163], v221 offset:0\n\tds_read_b128 v[168:171], v221 offset:32\n\tglobal_load_lds_dwordx4 v7, s[98:99] offset:2048\n\tv_mfma_f32_32x32x16_bf16 v[92:107], v[84:87], v[240:243], v[92:107]\n\tds_read_b128 v[176:179], v221 offset:64\n\tds_read_b128 v[184:187], v221 offset:96\n\tds_read_b128 v[200:203], v221 offset:128\n\ts_add_u32 m0, s64, 76800\n\ts_add_u32 s98, s98, 0xd800\n\ts_addc_u32 s99, s99, 0\n\tglobal_load_lds_dwordx4 v5, s[98:99] offset:0\n\tv_mfma_f32_32x32x16_bf16 v[108:123], v[84:87], v[244:247], v[108:123]\n\tglobal_load_lds_dwordx4 v6, s[98:99] offset:1024\n\tglobal_load_lds_dwordx4 v7, s[98:99] offset:2048\n\ts_waitcnt vmcnt(12) lgkmcnt(5)\n\ts_barrier\n\tv_mfma_f32_32x32x16_bf16 v[92:107], v[88:91], v[248:251], v[92:107]\n\tds_read_b64_tr_b16 v[64:65], v13 offset:0\n\ts_add_u32 m0, s64, 12288\n\ts_sub_u32 s98, s98, 0x28800\n\ts_subb_u32 s99, s99, 0\n\tds_read_b64_tr_b16 v[66:67], v13 offset:36864\n\tv_bfe_u32 v220, v0, 4, 1\n\tv_lshrrev_b32 v223, 5, v0\n\tv_mul_u32_u24 v223, 9216, v223\n\tv_lshl_add_u32 v220, v220, 8, v223\n\tv_bfe_u32 v223, v0, 1, 3\n\tv_lshl_add_u32 v220, v223, 5, v220\n\tv_and_b32 v223, 1, v0\n\tv_lshl_add_u32 v220, v223, 4, v220\n\ts_mul_i32 s95, s62, 512\n\tv_add_u32 v220, s95, v220\n\tv_mfma_f32_32x32x16_bf16 v[108:123], v[88:91], v[252:255], v[108:123]\n\tglobal_load_lds_dwordx4 v5, s[98:99] offset:0\n\tds_read_b64_tr_b16 v[68:69], v13 offset:1024\n\tglobal_load_lds_dwordx4 v6, s[98:99] offset:1024\n\tv_mfma_f32_32x32x16_bf16 v[92:107], v[44:47], v[156:159], v[92:107]\n\tds_read_b64_tr_b16 v[70:71], v13 offset:37888\n\tglobal_load_lds_dwordx4 v7, s[98:99] offset:2048\n\tds_read_b64_tr_b16 v[72:73], v13 offset:2048\n\tv_bfe_u32 v221, v0, 4, 1\n\ts_mul_i32 s95, s62, 32\n\tv_lshl_add_u32 v221, v221, 4, s95\n\tv_lshrrev_b32 v223, 5, v0\n\tv_lshl_add_u32 v221, v223, 3, v221\n\tv_and_b32 v223, 1, v0\n\tv_lshl_add_u32 v221, v223, 2, v221\n\tv_bfe_u32 v223, v0, 1, 3\n\tv_mul_u32_u24 v223, 36864, v223\n\tv_lshl_add_u32 v221, v221, 1, v223\n\ts_waitcnt lgkmcnt(9)\n\tv_mfma_f32_32x32x16_bf16 v[108:123], v[44:47], v[160:163], v[108:123]\n\tds_read_b64_tr_b16 v[74:75], v13 offset:38912\n\ts_add_u32 m0, s64, 15360\n\ts_add_u32 s98, s98, 0xd800\n\ts_addc_u32 s99, s99, 0\n\tv_mfma_f32_32x32x16_bf16 v[92:107], v[48:51], v[164:167], v[92:107]\n\tds_read_b64_tr_b16 v[76:77], v13 offset:3072\n\tglobal_load_lds_dwordx4 v5, s[98:99] offset:0\n\tds_read_b64_tr_b16 v[78:79], v13 offset:39936\n\tglobal_load_lds_dwordx4 v6, s[98:99] offset:1024\n\ts_waitcnt lgkmcnt(11)\n\tv_mfma_f32_32x32x16_bf16 v[108:123], v[48:51], v[168:171], v[108:123]\n\tds_read_b64_tr_b16 v[80:81], v13 offset:4096\n\tglobal_load_lds_dwordx4 v7, s[98:99] offset:2048\n\tv_bfe_u32 v33, v0, 1, 3\n\tv_lshrrev_b32 v223, 4, v0\n\tv_mul_u32_u24 v223, 9216, v223\n\tv_lshl_add_u32 v33, v33, 5, v223\n\tv_and_b32 v223, 1, v0\n\tv_lshl_add_u32 v33, v223, 4, v33\n\ts_lshr_b32 s92, s62, 1\n\ts_mul_i32 s93, s92, 512\n\ts_and_b32 s91, s62, 1\n\ts_mul_i32 s92, s91, 256\n\ts_add_u32 s94, s93, s92\n\ts_add_u32 s95, s94, 8192\n\tv_add_u32 v33, s95, v33\n\tv_mfma_f32_32x32x16_bf16 v[92:107], v[52:55], v[172:175], v[92:107]\n\tds_read_b64_tr_b16 v[82:83], v13 offset:40960\n\ts_add_u32 m0, s64, 79872\n\ts_add_u32 s98, s98, 0x28800\n\ts_addc_u32 s99, s99, 0\n\tds_read_b64_tr_b16 v[84:85], v13 offset:5120\n\ts_waitcnt lgkmcnt(13)\n\tv_mfma_f32_32x32x16_bf16 v[108:123], v[52:55], v[176:179], v[108:123]\n\tds_read_b64_tr_b16 v[86:87], v13 offset:41984\n\tglobal_load_lds_dwordx4 v5, s[98:99] offset:0\n\tv_lshrrev_b32 v34, 5, v0\n\ts_mul_i32 s95, s62, 16\n\tv_lshl_add_u32 v34, v34, 3, s95\n\tv_and_b32 v223, 1, v0\n\tv_lshl_add_u32 v34, v223, 2, v34\n\tv_add_u32 v34, 512, v34\n\tv_bfe_u32 v223, v0, 1, 3\n\tv_mul_u32_u24 v223, 36864, v223\n\tv_lshl_add_u32 v34, v34, 1, v223\n\tv_mfma_f32_32x32x16_bf16 v[92:107], v[56:59], v[180:183], v[92:107]\n\tglobal_load_lds_dwordx4 v6, s[98:99] offset:1024\n\ts_waitcnt lgkmcnt(13)\n\tv_mfma_f32_32x32x16_bf16 v[108:123], v[56:59], v[184:187], v[108:123]\n\tglobal_load_lds_dwordx4 v7, s[98:99] offset:2048\n\ts_add_u32 m0, s64, 82944\n\ts_add_u32 s98, s98, 0xd800\n\ts_addc_u32 s99, s99, 0\n\ts_mul_i32 s93, s60, 8\n\ts_add_u32 s94, s93, s61\n\ts_mul_i32 s95, s94, 73728\n\ts_add_u32 s70, s95, 0\n\tv_mfma_f32_32x32x16_bf16 v[92:107], v[60:63], v[196:199], v[92:107]\n\tglobal_load_lds_dwordx4 v5, s[98:99] offset:0\n\ts_mul_i32 s94, s61, 256\n\ts_add_u32 s95, s60, s94\n\ts_mul_i32 s71, s95, 1152\n\tglobal_load_lds_dwordx4 v6, s[98:99] offset:1024\n\ts_waitcnt lgkmcnt(12)\n\tv_mfma_f32_32x32x16_bf16 v[108:123], v[60:63], v[200:203], v[108:123]\n\ts_mov_b32 s96, 0x00003f80\n\ts_mov_b64 s[52:53], %[out]\n\ts_and_b32 s53, s53, 0xffff\n\ts_mov_b32 s54, 2359296\n\ts_mov_b32 s55, 0x00020000\n\tglobal_load_lds_dwordx4 v7, s[98:99] offset:2048\n\ts_waitcnt vmcnt(18) lgkmcnt(0)\n\ts_barrier\n\tv_mfma_f32_32x32x16_bf16 v[124:139], v[64:67], a[0:3], 0\n\tds_read_b64_tr_b16 v[88:89], v13 offset:6144\n\ts_add_u32 m0, s64, 86016\n\ts_add_u32 s98, s98, 0xd800\n\ts_addc_u32 s99, s99, 0\n\tds_read_b64_tr_b16 v[90:91], v13 offset:43008\n\tv_mfma_f32_32x32x16_bf16 v[140:155], v[64:67], a[4:7], 0\n\tv_cvt_pk_bf16_f32 v188, v92, v93\n\tds_read_b64_tr_b16 v[44:45], v13 offset:7168\n\tglobal_load_lds_dwordx4 v5, s[98:99] offset:0\n\tv_mfma_f32_32x32x16_bf16 v[124:139], v[68:71], a[8:11], v[124:139]\n\tds_read_b64_tr_b16 v[46:47], v13 offset:44032\n\tv_cvt_pk_bf16_f32 v189, v94, v95\n\tv_cvt_pk_bf16_f32 v190, v96, v97\n\tds_read_b64_tr_b16 v[48:49], v13 offset:8192\n\tv_mfma_f32_32x32x16_bf16 v[140:155], v[68:71], a[12:15], v[140:155]\n\tglobal_load_lds_dwordx4 v6, s[98:99] offset:1024\n\tv_cvt_pk_bf16_f32 v191, v98, v99\n\tds_read_b64_tr_b16 v[50:51], v13 offset:45056\n\tv_mfma_f32_32x32x16_bf16 v[124:139], v[72:75], a[16:19], v[124:139]\n\tds_read_b64_tr_b16 v[52:53], v13 offset:9216\n\tglobal_load_lds_dwordx4 v7, s[98:99] offset:2048\n\tds_read_b64_tr_b16 v[54:55], v13 offset:46080\n\tv_mfma_f32_32x32x16_bf16 v[140:155], v[72:75], a[20:23], v[140:155]\n\tds_read_b64_tr_b16 v[56:57], v13 offset:10240\n\tbuffer_store_dwordx4 v[188:191], v35, s[56:59], s65 offen offset:0 sc1\n\tv_mfma_f32_32x32x16_bf16 v[124:139], v[76:79], a[24:27], v[124:139]\n\tds_read_b64_tr_b16 v[58:59], v13 offset:47104\n\ts_add_u32 m0, s64, 89088\n\ts_add_u32 s98, s98, 0xd800\n\ts_addc_u32 s99, s99, 0\n\tds_read_b64_tr_b16 v[60:61], v13 offset:11264\n\tv_mfma_f32_32x32x16_bf16 v[140:155], v[76:79], a[28:31], v[140:155]\n\tds_read_b64_tr_b16 v[62:63], v13 offset:48128\n\tglobal_load_lds_dwordx4 v5, s[98:99] offset:0\n\tv_mfma_f32_32x32x16_bf16 v[124:139], v[80:83], a[32:35], v[124:139]\n\tglobal_load_lds_dwordx4 v6, s[98:99] offset:1024\n\tv_mfma_f32_32x32x16_bf16 v[140:155], v[80:83], a[36:39], v[140:155]\n\tv_mfma_f32_32x32x16_bf16 v[124:139], v[84:87], a[40:43], v[124:139]\n\tglobal_load_lds_dwordx4 v7, s[98:99] offset:2048\n\tv_mfma_f32_32x32x16_bf16 v[140:155], v[84:87], a[44:47], v[140:155]\n\ts_waitcnt vmcnt(13) lgkmcnt(0)\n\ts_barrier\n\tv_mfma_f32_32x32x16_bf16 v[124:139], v[88:91], a[48:51], v[124:139]\n\tds_read_b64_tr_b16 v[64:65], v13 offset:12288\n\ts_add_u32 m0, s64, 0\n\ts_add_u32 s98, s86, 0x100\n\ts_add_u32 s98, s98, s24\n\ts_addc_u32 s99, s25, 0\n\tds_read_b64_tr_b16 v[66:67], v13 offset:49152\n\tglobal_load_lds_dwordx4 v5, s[98:99] offset:0\n\tv_mfma_f32_32x32x16_bf16 v[140:155], v[88:91], a[52:55], v[140:155]\n\tds_read_b64_tr_b16 v[68:69], v13 offset:13312\n\tglobal_load_lds_dwordx4 v6, s[98:99] offset:1024\n\tv_mfma_f32_32x32x16_bf16 v[124:139], v[44:47], a[56:59], v[124:139]\n\tds_read_b64_tr_b16 v[70:71], v13 offset:50176\n\tv_cvt_pk_bf16_f32 v192, v100, v101\n\tglobal_load_lds_dwordx4 v7, s[98:99] offset:2048\n\tv_mfma_f32_32x32x16_bf16 v[140:155], v[44:47], a[60:63], v[140:155]\n\tds_read_b64_tr_b16 v[72:73], v13 offset:14336\n\tv_cvt_pk_bf16_f32 v193, v102, v103\n\tds_read_b64_tr_b16 v[74:75], v13 offset:51200\n\tv_cvt_pk_bf16_f32 v194, v104, v105\n\ts_add_u32 m0, s64, 3072\n\tv_cvt_pk_bf16_f32 v195, v106, v107\n\ts_add_u32 s98, s98, 0xd800\n\ts_addc_u32 s99, s99, 0\n\tv_mfma_f32_32x32x16_bf16 v[124:139], v[48:51], a[64:67], v[124:139]\n\tds_read_b64_tr_b16 v[76:77], v13 offset:15360\n\tglobal_load_lds_dwordx4 v5, s[98:99] offset:0\n\tds_read_b64_tr_b16 v[78:79], v13 offset:52224\n\tv_mfma_f32_32x32x16_bf16 v[140:155], v[48:51], a[68:71], v[140:155]\n\tglobal_load_lds_dwordx4 v6, s[98:99] offset:1024\n\tds_read_b64_tr_b16 v[80:81], v13 offset:16384\n\ts_add_u32 m0, s64, 6144\n\tbuffer_store_dwordx4 v[192:195], v35, s[56:59], s65 offen offset:256 sc1\n\tv_mfma_f32_32x32x16_bf16 v[124:139], v[52:55], a[72:75], v[124:139]\n\tds_read_b64_tr_b16 v[82:83], v13 offset:53248\n\ts_add_u32 s98, s81, 0x200\n\ts_add_u32 s98, s98, s48\n\ts_addc_u32 s99, s49, 0\n\tds_read_b64_tr_b16 v[84:85], v13 offset:17408\n\tv_mfma_f32_32x32x16_bf16 v[140:155], v[52:55], a[76:79], v[140:155]\n\tds_read_b64_tr_b16 v[86:87], v13 offset:54272\n\tglobal_load_lds_dwordx4 v5, s[98:99] offset:0\n\tv_mfma_f32_32x32x16_bf16 v[124:139], v[56:59], a[80:83], v[124:139]\n\tglobal_load_lds_dwordx4 v6, s[98:99] offset:1024\n\tv_mfma_f32_32x32x16_bf16 v[140:155], v[56:59], a[84:87], v[140:155]\n\tglobal_load_lds_dwordx4 v7, s[98:99] offset:2048\n\ts_add_u32 m0, s64, 9216\n\tv_mfma_f32_32x32x16_bf16 v[124:139], v[60:63], a[88:91], v[124:139]\n\ts_add_u32 s98, s98, 0xd800\n\ts_addc_u32 s99, s99, 0\n\tglobal_load_lds_dwordx4 v5, s[98:99] offset:0\n\tv_mfma_f32_32x32x16_bf16 v[140:155], v[60:63], a[92:95], v[140:155]\n\tglobal_load_lds_dwordx4 v6, s[98:99] offset:1024\n\tglobal_load_lds_dwordx4 v7, s[98:99] offset:2048\n\ts_waitcnt lgkmcnt(0)\n\ts_barrier\n\tv_mfma_f32_32x32x16_bf16 v[124:139], v[64:67], a[96:99], v[124:139]\n\tds_read_b64_tr_b16 v[88:89], v14 offset:0\n\ts_add_u32 m0, s64, 12288\n\ts_add_u32 s98, s98, 0xd800\n\ts_addc_u32 s99, s99, 0\n\tds_read_b64_tr_b16 v[90:91], v14 offset:36864\n\tv_mfma_f32_32x32x16_bf16 v[140:155], v[64:67], a[100:103], v[140:155]\n\tds_read_b64_tr_b16 v[44:45], v14 offset:1024\n\tglobal_load_lds_dwordx4 v5, s[98:99] offset:0\n\tv_mfma_f32_32x32x16_bf16 v[124:139], v[68:71], a[104:107], v[124:139]\n\tds_read_b64_tr_b16 v[46:47], v14 offset:37888\n\tv_cvt_pk_bf16_f32 v188, v108, v109\n\tds_read_b64_tr_b16 v[48:49], v14 offset:2048\n\tv_cvt_pk_bf16_f32 v189, v110, v111\n\tv_mfma_f32_32x32x16_bf16 v[140:155], v[68:71], a[108:111], v[140:155]\n\tglobal_load_lds_dwordx4 v6, s[98:99] offset:1024\n\tds_read_b64_tr_b16 v[50:51], v14 offset:38912\n\tv_cvt_pk_bf16_f32 v190, v112, v113\n\tv_mfma_f32_32x32x16_bf16 v[124:139], v[72:75], a[112:115], v[124:139]\n\tds_read_b64_tr_b16 v[52:53], v14 offset:3072\n\tv_cvt_pk_bf16_f32 v191, v114, v115\n\tglobal_load_lds_dwordx4 v7, s[98:99] offset:2048\n\tv_mfma_f32_32x32x16_bf16 v[140:155], v[72:75], a[116:119], v[140:155]\n\tds_read_b64_tr_b16 v[54:55], v14 offset:39936\n\tds_read_b64_tr_b16 v[56:57], v14 offset:4096\n\tbuffer_store_dwordx4 v[188:191], v35, s[56:59], s66 offen offset:0 sc1\n\tv_mfma_f32_32x32x16_bf16 v[124:139], v[76:79], a[120:123], v[124:139]\n\tds_read_b64_tr_b16 v[58:59], v14 offset:40960\n\ts_add_u32 m0, s64, 15360\n\ts_add_u32 s98, s98, 0xd800\n\ts_addc_u32 s99, s99, 0\n\tds_read_b64_tr_b16 v[60:61], v14 offset:5120\n\tv_mfma_f32_32x32x16_bf16 v[140:155], v[76:79], a[124:127], v[140:155]\n\tds_read_b64_tr_b16 v[62:63], v14 offset:41984\n\tglobal_load_lds_dwordx4 v5, s[98:99] offset:0\n\tv_mfma_f32_32x32x16_bf16 v[124:139], v[80:83], a[128:131], v[124:139]\n\tglobal_load_lds_dwordx4 v6, s[98:99] offset:1024\n\tv_mfma_f32_32x32x16_bf16 v[140:155], v[80:83], a[132:135], v[140:155]\n\tv_mfma_f32_32x32x16_bf16 v[124:139], v[84:87], a[136:139], v[124:139]\n\tglobal_load_lds_dwordx4 v7, s[98:99] offset:2048\n\tv_mfma_f32_32x32x16_bf16 v[140:155], v[84:87], a[140:143], v[140:155]\n\ts_waitcnt vmcnt(26) lgkmcnt(0)\n\ts_barrier\n\tv_mfma_f32_32x32x16_bf16 v[124:139], v[88:91], a[144:147], v[124:139]\n\tds_read_b64_tr_b16 v[64:65], v14 offset:6144\n\ts_add_u32 m0, s64, 73728\n\ts_add_u32 s98, s98, 0xd800\n\ts_addc_u32 s99, s99, 0\n\tds_read_b64_tr_b16 v[66:67], v14 offset:43008\n\tv_mfma_f32_32x32x16_bf16 v[140:155], v[88:91], a[148:151], v[140:155]\n\tds_read_b64_tr_b16 v[68:69], v14 offset:7168\n\tglobal_load_lds_dwordx4 v5, s[98:99] offset:0\n\tv_mfma_f32_32x32x16_bf16 v[124:139], v[44:47], a[152:155], v[124:139]\n\tds_read_b64_tr_b16 v[70:71], v14 offset:44032\n\tv_cvt_pk_bf16_f32 v192, v116, v117\n\tds_read_b64_tr_b16 v[72:73], v14 offset:8192\n\tv_cvt_pk_bf16_f32 v193, v118, v119\n\tv_mfma_f32_32x32x16_bf16 v[140:155], v[44:47], a[156:159], v[140:155]\n\tglobal_load_lds_dwordx4 v6, s[98:99] offset:1024\n\tds_read_b64_tr_b16 v[74:75], v14 offset:45056\n\tv_cvt_pk_bf16_f32 v194, v120, v121\n\tv_mfma_f32_32x32x16_bf16 v[124:139], v[48:51], a[160:163], v[124:139]\n\tds_read_b64_tr_b16 v[76:77], v14 offset:9216\n\tv_cvt_pk_bf16_f32 v195, v122, v123\n\tglobal_load_lds_dwordx4 v7, s[98:99] offset:2048\n\tv_mfma_f32_32x32x16_bf16 v[140:155], v[48:51], a[164:167], v[140:155]\n\tds_read_b64_tr_b16 v[78:79], v14 offset:46080\n\tds_read_b64_tr_b16 v[80:81], v14 offset:10240\n\tbuffer_store_dwordx4 v[192:195], v35, s[56:59], s66 offen offset:256 sc1\n\tv_mfma_f32_32x32x16_bf16 v[124:139], v[52:55], a[168:171], v[124:139]\n\tds_read_b64_tr_b16 v[82:83], v14 offset:47104\n\ts_add_u32 m0, s64, 76800\n\ts_add_u32 s98, s98, 0xd800\n\ts_addc_u32 s99, s99, 0\n\tds_read_b64_tr_b16 v[84:85], v14 offset:11264\n\tv_mfma_f32_32x32x16_bf16 v[140:155], v[52:55], a[172:175], v[140:155]\n\tds_read_b64_tr_b16 v[86:87], v14 offset:48128\n\tglobal_load_lds_dwordx4 v5, s[98:99] offset:0\n\tv_mfma_f32_32x32x16_bf16 v[124:139], v[56:59], a[176:179], v[124:139]\n\tglobal_load_lds_dwordx4 v6, s[98:99] offset:1024\n\tv_mfma_f32_32x32x16_bf16 v[140:155], v[56:59], a[180:183], v[140:155]\n\tv_mfma_f32_32x32x16_bf16 v[124:139], v[60:63], a[184:187], v[124:139]\n\tglobal_load_lds_dwordx4 v7, s[98:99] offset:2048\n\tv_mfma_f32_32x32x16_bf16 v[140:155], v[60:63], a[188:191], v[140:155]\n\ts_waitcnt vmcnt(26) lgkmcnt(0)\n\ts_barrier\n\tv_mfma_f32_32x32x16_bf16 v[124:139], v[64:67], a[192:195], v[124:139]\n\tds_read_b64_tr_b16 v[88:89], v14 offset:12288\n\ts_add_u32 m0, s64, 79872\n\ts_add_u32 s98, s98, 0xd800\n\ts_addc_u32 s99, s99, 0\n\tds_read_b64_tr_b16 v[90:91], v14 offset:49152\n\tv_mfma_f32_32x32x16_bf16 v[140:155], v[64:67], a[196:199], v[140:155]\n\tds_read_b64_tr_b16 v[44:45], v14 offset:13312\n\tglobal_load_lds_dwordx4 v5, s[98:99] offset:0\n\tv_mfma_f32_32x32x16_bf16 v[124:139], v[68:71], a[200:203], v[124:139]\n\tds_read_b64_tr_b16 v[46:47], v14 offset:50176\n\tds_read_b64_tr_b16 v[48:49], v14 offset:14336\n\tglobal_load_lds_dwordx4 v6, s[98:99] offset:1024\n\tv_mfma_f32_32x32x16_bf16 v[140:155], v[68:71], a[204:207], v[140:155]\n\tds_read_b64_tr_b16 v[50:51], v14 offset:51200\n\tv_mfma_f32_32x32x16_bf16 v[124:139], v[72:75], a[208:211], v[124:139]\n\tds_read_b64_tr_b16 v[52:53], v14 offset:15360\n\tglobal_load_lds_dwordx4 v7, s[98:99] offset:2048\n\tds_read_b64_tr_b16 v[54:55], v14 offset:52224\n\tv_mfma_f32_32x32x16_bf16 v[140:155], v[72:75], a[212:215], v[140:155]\n\tds_read_b64_tr_b16 v[56:57], v14 offset:16384\n\tv_mfma_f32_32x32x16_bf16 v[124:139], v[76:79], a[216:219], v[124:139]\n\tds_read_b64_tr_b16 v[58:59], v14 offset:53248\n\ts_add_u32 m0, s64, 82944\n\ts_add_u32 s98, s98, 0xd800\n\ts_addc_u32 s99, s99, 0\n\tds_read_b64_tr_b16 v[60:61], v14 offset:17408\n\tv_mfma_f32_32x32x16_bf16 v[140:155], v[76:79], a[220:223], v[140:155]\n\tds_read_b64_tr_b16 v[62:63], v14 offset:54272\n\tglobal_load_lds_dwordx4 v5, s[98:99] offset:0\n\tv_mfma_f32_32x32x16_bf16 v[124:139], v[80:83], a[224:227], v[124:139]\n\tglobal_load_lds_dwordx4 v6, s[98:99] offset:1024\n\tv_mfma_f32_32x32x16_bf16 v[140:155], v[80:83], a[228:231], v[140:155]\n\tv_mfma_f32_32x32x16_bf16 v[124:139], v[84:87], a[232:235], v[124:139]\n\tglobal_load_lds_dwordx4 v7, s[98:99] offset:2048\n\tv_mfma_f32_32x32x16_bf16 v[140:155], v[84:87], a[236:239], v[140:155]\n\ts_waitcnt vmcnt(27) lgkmcnt(0)\n\ts_barrier\n\tv_mfma_f32_32x32x16_bf16 v[124:139], v[88:91], a[240:243], v[124:139]\n\tds_read_b64_tr_b16 v[64:65], v13 offset:0\n\ts_add_u32 m0, s64, 86016\n\ts_add_u32 s98, s98, 0xd800\n\ts_addc_u32 s99, s99, 0\n\tds_read_b64_tr_b16 v[66:67], v13 offset:36864\n\tv_mfma_f32_32x32x16_bf16 v[140:155], v[88:91], a[244:247], v[140:155]\n\tglobal_load_lds_dwordx4 v5, s[98:99] offset:0\n\tds_read_b64_tr_b16 v[68:69], v13 offset:1024\n\tv_mfma_f32_32x32x16_bf16 v[124:139], v[44:47], a[248:251], v[124:139]\n\tds_read_b64_tr_b16 v[70:71], v13 offset:37888\n\tglobal_load_lds_dwordx4 v6, s[98:99] offset:1024\n\tds_read_b64_tr_b16 v[72:73], v13 offset:2048\n\tv_mfma_f32_32x32x16_bf16 v[140:155], v[44:47], a[252:255], v[140:155]\n\tds_read_b64_tr_b16 v[74:75], v13 offset:38912\n\tglobal_load_lds_dwordx4 v7, s[98:99] offset:2048\n\tv_mfma_f32_32x32x16_bf16 v[124:139], v[48:51], v[224:227], v[124:139]\n\tds_read_b64_tr_b16 v[76:77], v13 offset:3072\n\tds_read_b64_tr_b16 v[78:79], v13 offset:39936\n\tv_mfma_f32_32x32x16_bf16 v[140:155], v[48:51], v[228:231], v[140:155]\n\ts_add_u32 m0, s64, 89088\n\ts_add_u32 s98, s98, 0xd800\n\ts_addc_u32 s99, s99, 0\n\tds_read_b64_tr_b16 v[80:81], v13 offset:4096\n\tv_mfma_f32_32x32x16_bf16 v[124:139], v[52:55], v[232:235], v[124:139]\n\tds_read_b64_tr_b16 v[82:83], v13 offset:40960\n\tglobal_load_lds_dwordx4 v5, s[98:99] offset:0\n\tv_mfma_f32_32x32x16_bf16 v[140:155], v[52:55], v[236:239], v[140:155]\n\tglobal_load_lds_dwordx4 v6, s[98:99] offset:1024\n\tv_mfma_f32_32x32x16_bf16 v[124:139], v[56:59], v[240:243], v[124:139]\n\tglobal_load_lds_dwordx4 v7, s[98:99] offset:2048\n\tv_mfma_f32_32x32x16_bf16 v[140:155], v[56:59], v[244:247], v[140:155]\n\ts_waitcnt vmcnt(15) lgkmcnt(0)\n\ts_barrier\n\ts_mul_i32 s93, s60, 320\n\ts_mul_i32 s92, s61, 8\n\ts_add_u32 s94, s93, s92\n\ts_add_u32 s95, s94, 4096\n\ts_add_u32 s83, s95, 24117248\n\tv_mov_b32 v30, s88\n\tv_mov_b32 v31, s89\n\tbuffer_store_dwordx2 v[30:31], v42, s[56:59], s83 offen sc1\n\tv_mfma_f32_32x32x16_bf16 v[124:139], v[60:63], v[248:251], v[124:139]\n\tds_read_b64_tr_b16 v[84:85], v13 offset:6144\n\ts_add_u32 m0, s64, 0\n\ts_add_u32 s98, s98, 0xd800\n\ts_addc_u32 s99, s99, 0\n\tds_read_b64_tr_b16 v[86:87], v13 offset:43008\n\tv_mfma_f32_32x32x16_bf16 v[140:155], v[60:63], v[252:255], v[140:155]\n\tds_read_b64_tr_b16 v[88:89], v13 offset:7168\n\tglobal_load_lds_dwordx4 v5, s[98:99] offset:0\n\tv_mfma_f32_32x32x16_bf16 v[124:139], v[64:67], v[156:159], v[124:139]\n\tds_read_b64_tr_b16 v[90:91], v13 offset:44032\n\tds_read_b64_tr_b16 v[44:45], v13 offset:8192\n\tglobal_load_lds_dwordx4 v6, s[98:99] offset:1024\n\tv_mfma_f32_32x32x16_bf16 v[140:155], v[64:67], v[160:163], v[140:155]\n\tds_read_b64_tr_b16 v[46:47], v13 offset:45056\n\tv_mfma_f32_32x32x16_bf16 v[124:139], v[68:71], v[164:167], v[124:139]\n\tds_read_b64_tr_b16 v[48:49], v13 offset:9216\n\tglobal_load_lds_dwordx4 v7, s[98:99] offset:2048\n\tds_read_b64_tr_b16 v[50:51], v13 offset:46080\n\tv_mfma_f32_32x32x16_bf16 v[140:155], v[68:71], v[168:171], v[140:155]\n\tds_read_b64_tr_b16 v[52:53], v13 offset:10240\n\tv_mfma_f32_32x32x16_bf16 v[124:139], v[72:75], v[172:175], v[124:139]\n\tds_read_b64_tr_b16 v[54:55], v13 offset:47104\n\ts_add_u32 m0, s64, 3072\n\ts_add_u32 s98, s98, 0xd800\n\ts_addc_u32 s99, s99, 0\n\tds_read_b64_tr_b16 v[56:57], v13 offset:11264\n\tv_mfma_f32_32x32x16_bf16 v[140:155], v[72:75], v[176:179], v[140:155]\n\tds_read_b64_tr_b16 v[58:59], v13 offset:48128\n\tglobal_load_lds_dwordx4 v5, s[98:99] offset:0\n\tv_mfma_f32_32x32x16_bf16 v[124:139], v[76:79], v[180:183], v[124:139]\n\tglobal_load_lds_dwordx4 v6, s[98:99] offset:1024\n\tv_mfma_f32_32x32x16_bf16 v[140:155], v[76:79], v[184:187], v[140:155]\n\tv_mfma_f32_32x32x16_bf16 v[124:139], v[80:83], v[196:199], v[124:139]\n\tglobal_load_lds_dwordx4 v7, s[98:99] offset:2048\n\tv_mfma_f32_32x32x16_bf16 v[140:155], v[80:83], v[200:203], v[140:155]\n\ts_waitcnt lgkmcnt(0)\n\ts_barrier\n\tv_mfma_f32_32x32x16_bf16 v[92:107], v[84:87], a[0:3], 0\n\tds_read_b64_tr_b16 v[60:61], v13 offset:12288\n\ts_add_u32 m0, s64, 6144\n\ts_add_u32 s98, s86, 0x200\n\ts_add_u32 s98, s98, s24\n\ts_addc_u32 s99, s25, 0\n\tds_read_b64_tr_b16 v[62:63], v13 offset:49152\n\tv_mfma_f32_32x32x16_bf16 v[108:123], v[84:87], a[4:7], 0\n\tv_cvt_pk_bf16_f32 v188, v124, v125\n\tds_read_b64_tr_b16 v[64:65], v13 offset:13312\n\tv_cvt_pk_bf16_f32 v189, v126, v127\n\tv_mfma_f32_32x32x16_bf16 v[92:107], v[88:91], a[8:11], v[92:107]\n\tds_read_b64_tr_b16 v[66:67], v13 offset:50176\n\tglobal_load_lds_dwordx4 v5, s[98:99] offset:0\n\tv_cvt_pk_bf16_f32 v190, v128, v129\n\tv_mfma_f32_32x32x16_bf16 v[108:123], v[88:91], a[12:15], v[108:123]\n\tds_read_b64_tr_b16 v[68:69], v13 offset:14336\n\tv_cvt_pk_bf16_f32 v191, v130, v131\n\tds_read_b64_tr_b16 v[70:71], v13 offset:51200\n\tv_mfma_f32_32x32x16_bf16 v[92:107], v[44:47], a[16:19], v[92:107]\n\tds_read_b64_tr_b16 v[72:73], v13 offset:15360\n\tglobal_load_lds_dwordx4 v6, s[98:99] offset:1024\n\tds_read_b64_tr_b16 v[74:75], v13 offset:52224\n\tv_mfma_f32_32x32x16_bf16 v[108:123], v[44:47], a[20:23], v[108:123]\n\tds_read_b64_tr_b16 v[76:77], v13 offset:16384\n\tglobal_load_lds_dwordx4 v7, s[98:99] offset:2048\n\tv_mfma_f32_32x32x16_bf16 v[92:107], v[48:51], a[24:27], v[92:107]\n\tds_read_b64_tr_b16 v[78:79], v13 offset:53248\n\tds_read_b64_tr_b16 v[80:81], v13 offset:17408\n\tbuffer_store_dwordx4 v[188:191], v35, s[56:59], s65 offen offset:2048 sc1\n\tv_mfma_f32_32x32x16_bf16 v[108:123], v[48:51], a[28:31], v[108:123]\n\tds_read_b64_tr_b16 v[82:83], v13 offset:54272\n\ts_add_u32 m0, s64, 9216\n\ts_add_u32 s98, s98, 0xd800\n\ts_addc_u32 s99, s99, 0\n\tv_mfma_f32_32x32x16_bf16 v[92:107], v[52:55], a[32:35], v[92:107]\n\tglobal_load_lds_dwordx4 v5, s[98:99] offset:0\n\tv_mfma_f32_32x32x16_bf16 v[108:123], v[52:55], a[36:39], v[108:123]\n\tv_mfma_f32_32x32x16_bf16 v[92:107], v[56:59], a[40:43], v[92:107]\n\tglobal_load_lds_dwordx4 v6, s[98:99] offset:1024\n\tv_mfma_f32_32x32x16_bf16 v[108:123], v[56:59], a[44:47], v[108:123]\n\ts_waitcnt vmcnt(25) lgkmcnt(0)\n\ts_barrier\n\tv_mfma_f32_32x32x16_bf16 v[92:107], v[60:63], a[48:51], v[92:107]\n\tds_read_b64_tr_b16 v[84:85], v14 offset:0\n\ts_add_u32 m0, s64, 12288\n\ts_add_u32 s98, s81, 0x300\n\ts_add_u32 s98, s98, s48\n\ts_addc_u32 s99, s49, 0\n\tds_read_b64_tr_b16 v[86:87], v14 offset:36864\n\tv_mfma_f32_32x32x16_bf16 v[108:123], v[60:63], a[52:55], v[108:123]\n\tds_read_b64_tr_b16 v[88:89], v14 offset:1024\n\tglobal_load_lds_dwordx4 v5, s[98:99] offset:0\n\tv_mfma_f32_32x32x16_bf16 v[92:107], v[64:67], a[56:59], v[92:107]\n\tds_read_b64_tr_b16 v[90:91], v14 offset:37888\n\tv_cvt_pk_bf16_f32 v192, v132, v133\n\tds_read_b64_tr_b16 v[44:45], v14 offset:2048\n\tv_cvt_pk_bf16_f32 v193, v134, v135\n\tv_mfma_f32_32x32x16_bf16 v[108:123], v[64:67], a[60:63], v[108:123]\n\tds_read_b64_tr_b16 v[46:47], v14 offset:38912\n\tglobal_load_lds_dwordx4 v6, s[98:99] offset:1024\n\tv_cvt_pk_bf16_f32 v194, v136, v137\n\tv_mfma_f32_32x32x16_bf16 v[92:107], v[68:71], a[64:67], v[92:107]\n\tds_read_b64_tr_b16 v[48:49], v14 offset:3072\n\tv_cvt_pk_bf16_f32 v195, v138, v139\n\tds_read_b64_tr_b16 v[50:51], v14 offset:39936\n\tv_mfma_f32_32x32x16_bf16 v[108:123], v[68:71], a[68:71], v[108:123]\n\tglobal_load_lds_dwordx4 v7, s[98:99] offset:2048\n\tds_read_b64_tr_b16 v[52:53], v14 offset:4096\n\tv_mfma_f32_32x32x16_bf16 v[92:107], v[72:75], a[72:75], v[92:107]\n\tds_read_b64_tr_b16 v[54:55], v14 offset:40960\n\ts_add_u32 m0, s64, 15360\n\tds_read_b64_tr_b16 v[56:57], v14 offset:5120\n\ts_add_u32 s98, s98, 0xd800\n\ts_addc_u32 s99, s99, 0\n\tv_mfma_f32_32x32x16_bf16 v[108:123], v[72:75], a[76:79], v[108:123]\n\tds_read_b64_tr_b16 v[58:59], v14 offset:41984\n\tglobal_load_lds_dwordx4 v5, s[98:99] offset:0\n\tv_mfma_f32_32x32x16_bf16 v[92:107], v[76:79], a[80:83], v[92:107]\n\tglobal_load_lds_dwordx4 v6, s[98:99] offset:1024\n\tbuffer_store_dwordx4 v[192:195], v35, s[56:59], s65 offen offset:2304 sc1\n\tv_mfma_f32_32x32x16_bf16 v[108:123], v[76:79], a[84:87], v[108:123]\n\tv_mfma_f32_32x32x16_bf16 v[92:107], v[80:83], a[88:91], v[92:107]\n\tglobal_load_lds_dwordx4 v7, s[98:99] offset:2048\n\tv_mfma_f32_32x32x16_bf16 v[108:123], v[80:83], a[92:95], v[108:123]\n\ts_waitcnt vmcnt(26) lgkmcnt(0)\n\ts_barrier\n\tv_mfma_f32_32x32x16_bf16 v[92:107], v[84:87], a[96:99], v[92:107]\n\tds_read_b64_tr_b16 v[60:61], v14 offset:6144\n\ts_add_u32 m0, s64, 73728\n\ts_add_u32 s98, s98, 0xd800\n\ts_addc_u32 s99, s99, 0\n\tds_read_b64_tr_b16 v[62:63], v14 offset:43008\n\tv_mfma_f32_32x32x16_bf16 v[108:123], v[84:87], a[100:103], v[108:123]\n\ts_mul_i32 s94, s60, 320\n\ts_add_u32 s95, s94, 4096\n\ts_add_u32 s78, s95, 24117248\n\tds_read_b64_tr_b16 v[64:65], v14 offset:7168\n\tglobal_load_lds_dwordx4 v5, s[98:99] offset:0\n\tv_mfma_f32_32x32x16_bf16 v[92:107], v[88:91], a[104:107], v[92:107]\n\tv_and_b32 v32, 7, v0\n\tds_read_b64_tr_b16 v[66:67], v14 offset:44032\n\tv_cvt_pk_bf16_f32 v188, v140, v141\n\tv_lshlrev_b32 v32, 3, v32\n\tds_read_b64_tr_b16 v[68:69], v14 offset:8192\n\tv_cvt_pk_bf16_f32 v189, v142, v143\n\tv_mfma_f32_32x32x16_bf16 v[108:123], v[88:91], a[108:111], v[108:123]\n\tbuffer_load_dwordx2 v[222:223], v32, s[56:59], s78 offen sc1\n\tds_read_b64_tr_b16 v[70:71], v14 offset:45056\n\tv_cvt_pk_bf16_f32 v190, v144, v145\n\tv_mfma_f32_32x32x16_bf16 v[92:107], v[44:47], a[112:115], v[92:107]\n\tglobal_load_lds_dwordx4 v6, s[98:99] offset:1024\n\tds_read_b64_tr_b16 v[72:73], v14 offset:9216\n\tv_cvt_pk_bf16_f32 v191, v146, v147\n\tv_mfma_f32_32x32x16_bf16 v[108:123], v[44:47], a[116:119], v[108:123]\n\tds_read_b64_tr_b16 v[74:75], v14 offset:46080\n\tglobal_load_lds_dwordx4 v7, s[98:99] offset:2048\n\tds_read_b64_tr_b16 v[76:77], v14 offset:10240\n\tv_mfma_f32_32x32x16_bf16 v[92:107], v[48:51], a[120:123], v[92:107]\n\tds_read_b64_tr_b16 v[78:79], v14 offset:47104\n\ts_add_u32 m0, s64, 76800\n\ts_add_u32 s98, s98, 0xd800\n\ts_addc_u32 s99, s99, 0\n\tds_read_b64_tr_b16 v[80:81], v14 offset:11264\n\tv_mfma_f32_32x32x16_bf16 v[108:123], v[48:51], a[124:127], v[108:123]\n\tds_read_b64_tr_b16 v[82:83], v14 offset:48128\n\tglobal_load_lds_dwordx4 v5, s[98:99] offset:0\n\tv_mfma_f32_32x32x16_bf16 v[92:107], v[52:55], a[128:131], v[92:107]\n\tglobal_load_lds_dwordx4 v6, s[98:99] offset:1024\n\tv_mfma_f32_32x32x16_bf16 v[108:123], v[52:55], a[132:135], v[108:123]\n\tbuffer_store_dwordx4 v[188:191], v35, s[56:59], s66 offen offset:2048 sc1\n\tv_mfma_f32_32x32x16_bf16 v[92:107], v[56:59], a[136:139], v[92:107]\n\tglobal_load_lds_dwordx4 v7, s[98:99] offset:2048\n\tv_mfma_f32_32x32x16_bf16 v[108:123], v[56:59], a[140:143], v[108:123]\n\ts_waitcnt vmcnt(28) lgkmcnt(0)\n\ts_barrier\n\tv_mfma_f32_32x32x16_bf16 v[92:107], v[60:63], a[144:147], v[92:107]\n\tds_read_b64_tr_b16 v[84:85], v14 offset:12288\n\ts_add_u32 m0, s64, 79872\n\ts_add_u32 s98, s98, 0xd800\n\ts_addc_u32 s99, s99, 0\n\tds_read_b64_tr_b16 v[86:87], v14 offset:49152\n\tv_mfma_f32_32x32x16_bf16 v[108:123], v[60:63], a[148:151], v[108:123]\n\tds_read_b64_tr_b16 v[88:89], v14 offset:13312\n\tglobal_load_lds_dwordx4 v5, s[98:99] offset:0\n\tv_mfma_f32_32x32x16_bf16 v[92:107], v[64:67], a[152:155], v[92:107]\n\tds_read_b64_tr_b16 v[90:91], v14 offset:50176\n\tv_cvt_pk_bf16_f32 v192, v148, v149\n\tds_read_b64_tr_b16 v[44:45], v14 offset:14336\n\tv_cvt_pk_bf16_f32 v193, v150, v151\n\tv_mfma_f32_32x32x16_bf16 v[108:123], v[64:67], a[156:159], v[108:123]\n\tglobal_load_lds_dwordx4 v6, s[98:99] offset:1024\n\tds_read_b64_tr_b16 v[46:47], v14 offset:51200\n\tv_cvt_pk_bf16_f32 v194, v152, v153\n\tv_mfma_f32_32x32x16_bf16 v[92:107], v[68:71], a[160:163], v[92:107]\n\tds_read_b64_tr_b16 v[48:49], v14 offset:15360\n\tv_cvt_pk_bf16_f32 v195, v154, v155\n\tglobal_load_lds_dwordx4 v7, s[98:99] offset:2048\n\tv_mfma_f32_32x32x16_bf16 v[108:123], v[68:71], a[164:167], v[108:123]\n\tds_read_b64_tr_b16 v[50:51], v14 offset:52224\n\tds_read_b64_tr_b16 v[52:53], v14 offset:16384\n\tbuffer_store_dwordx4 v[192:195], v35, s[56:59], s66 offen offset:2304 sc1\n\tv_mfma_f32_32x32x16_bf16 v[92:107], v[72:75], a[168:171], v[92:107]\n\tds_read_b64_tr_b16 v[54:55], v14 offset:53248\n\ts_add_u32 m0, s64, 82944\n\ts_add_u32 s98, s98, 0xd800\n\ts_addc_u32 s99, s99, 0\n\tds_read_b64_tr_b16 v[56:57], v14 offset:17408\n\tv_mfma_f32_32x32x16_bf16 v[108:123], v[72:75], a[172:175], v[108:123]\n\tds_read_b64_tr_b16 v[58:59], v14 offset:54272\n\tglobal_load_lds_dwordx4 v5, s[98:99] offset:0\n\tv_mfma_f32_32x32x16_bf16 v[92:107], v[76:79], a[176:179], v[92:107]\n\tglobal_load_lds_dwordx4 v6, s[98:99] offset:1024\n\tv_mfma_f32_32x32x16_bf16 v[108:123], v[76:79], a[180:183], v[108:123]\n\tv_mfma_f32_32x32x16_bf16 v[92:107], v[80:83], a[184:187], v[92:107]\n\tglobal_load_lds_dwordx4 v7, s[98:99] offset:2048\n\tv_mfma_f32_32x32x16_bf16 v[108:123], v[80:83], a[188:191], v[108:123]\n\ts_waitcnt vmcnt(28) lgkmcnt(0)\n\ts_barrier\n\ts_waitcnt vmcnt(13)\n\tv_cmp_ne_u64 vcc, s[88:89], v[222:223]\n\ts_nop 3\n\ts_cbranch_vccz L_ok_r0_%=\n\ts_mov_b32 s83, 0\n\tL_sp_r0_%=:\n\ts_sleep 1\n\tbuffer_load_dwordx2 v[222:223], v32, s[56:59], s78 offen sc1\n\ts_waitcnt vmcnt(0)\n\tv_cmp_ne_u64 vcc, s[88:89], v[222:223]\n\ts_add_u32 s83, s83, 1\n\ts_nop 2\n\ts_cbranch_vccz L_ok_r0_%=\n\ts_cmp_lt_u32 s83, 16384\n\ts_cbranch_scc1 L_sp_r0_%=\n\tL_ok_r0_%=:\n\tv_mfma_f32_32x32x16_bf16 v[92:107], v[84:87], a[192:195], v[92:107]\n\ts_add_u32 s83, s70, 0\n\tbuffer_load_dwordx4 v[204:207], v220, s[56:59], s83 offen sc1\n\tds_read_b64_tr_b16 v[60:61], v13 offset:0\n\tv_mfma_f32_32x32x16_bf16 v[108:123], v[84:87], a[196:199], v[108:123]\n\tds_read_b64_tr_b16 v[62:63], v13 offset:36864\n\ts_add_u32 m0, s64, 86016\n\ts_add_u32 s98, s98, 0xd800\n\ts_addc_u32 s99, s99, 0\n\tds_read_b64_tr_b16 v[64:65], v13 offset:1024\n\tv_mfma_f32_32x32x16_bf16 v[92:107], v[88:91], a[200:203], v[92:107]\n\ts_add_u32 s83, s70, 18432\n\tbuffer_load_dwordx4 v[208:211], v220, s[56:59], s83 offen sc1\n\tds_read_b64_tr_b16 v[66:67], v13 offset:37888\n\tv_mfma_f32_32x32x16_bf16 v[108:123], v[88:91], a[204:207], v[108:123]\n\tglobal_load_lds_dwordx4 v5, s[98:99] offset:0\n\tds_read_b64_tr_b16 v[68:69], v13 offset:2048\n\tds_read_b64_tr_b16 v[70:71], v13 offset:38912\n\tv_mfma_f32_32x32x16_bf16 v[92:107], v[44:47], a[208:211], v[92:107]\n\ts_add_u32 s83, s70, 36864\n\tbuffer_load_dwordx4 v[212:215], v220, s[56:59], s83 offen sc1\n\tds_read_b64_tr_b16 v[72:73], v13 offset:3072\n\tv_mfma_f32_32x32x16_bf16 v[108:123], v[44:47], a[212:215], v[108:123]\n\tglobal_load_lds_dwordx4 v6, s[98:99] offset:1024\n\tds_read_b64_tr_b16 v[74:75], v13 offset:39936\n\tds_read_b64_tr_b16 v[76:77], v13 offset:4096\n\tv_mfma_f32_32x32x16_bf16 v[92:107], v[48:51], a[216:219], v[92:107]\n\ts_add_u32 s83, s70, 55296\n\tbuffer_load_dwordx4 v[216:219], v220, s[56:59], s83 offen sc1\n\ts_add_u32 s83, s71, 0\n\tbuffer_load_dwordx2 v[28:29], v221, s[40:43], s83 offen\n\tv_mfma_f32_32x32x16_bf16 v[108:123], v[48:51], a[220:223], v[108:123]\n\tds_read_b64_tr_b16 v[78:79], v13 offset:40960\n\tds_read_b64_tr_b16 v[80:81], v13 offset:5120\n\tds_read_b64_tr_b16 v[82:83], v13 offset:41984\n\tv_mfma_f32_32x32x16_bf16 v[92:107], v[52:55], a[224:227], v[92:107]\n\tglobal_load_lds_dwordx4 v7, s[98:99] offset:2048\n\ts_add_u32 m0, s64, 89088\n\tv_mfma_f32_32x32x16_bf16 v[108:123], v[52:55], a[228:231], v[108:123]\n\ts_add_u32 s98, s98, 0xd800\n\ts_addc_u32 s99, s99, 0\n\tglobal_load_lds_dwordx4 v5, s[98:99] offset:0\n\tv_mfma_f32_32x32x16_bf16 v[92:107], v[56:59], a[232:235], v[92:107]\n\tglobal_load_lds_dwordx4 v6, s[98:99] offset:1024\n\tv_mfma_f32_32x32x16_bf16 v[108:123], v[56:59], a[236:239], v[108:123]\n\tglobal_load_lds_dwordx4 v7, s[98:99] offset:2048\n\ts_waitcnt lgkmcnt(0)\n\ts_barrier\n\tv_mfma_f32_32x32x16_bf16 v[92:107], v[60:63], a[240:243], v[92:107]\n\tds_read_b64_tr_b16 v[84:85], v13 offset:6144\n\ts_add_u32 m0, s64, 0\n\ts_add_u32 s98, s98, 0xd800\n\ts_addc_u32 s99, s99, 0\n\tds_read_b64_tr_b16 v[86:87], v13 offset:43008\n\tv_mfma_f32_32x32x16_bf16 v[108:123], v[60:63], a[244:247], v[108:123]\n\tglobal_load_lds_dwordx4 v5, s[98:99] offset:0\n\tds_read_b64_tr_b16 v[88:89], v13 offset:7168\n\tv_mfma_f32_32x32x16_bf16 v[92:107], v[64:67], a[248:251], v[92:107]\n\tds_read_b64_tr_b16 v[90:91], v13 offset:44032\n\tglobal_load_lds_dwordx4 v6, s[98:99] offset:1024\n\tds_read_b64_tr_b16 v[44:45], v13 offset:8192\n\tv_mfma_f32_32x32x16_bf16 v[108:123], v[64:67], a[252:255], v[108:123]\n\tds_read_b64_tr_b16 v[46:47], v13 offset:45056\n\tglobal_load_lds_dwordx4 v7, s[98:99] offset:2048\n\tv_mfma_f32_32x32x16_bf16 v[92:107], v[68:71], v[224:227], v[92:107]\n\tds_read_b64_tr_b16 v[48:49], v13 offset:9216\n\tds_read_b64_tr_b16 v[50:51], v13 offset:46080\n\tv_mfma_f32_32x32x16_bf16 v[108:123], v[68:71], v[228:231], v[108:123]\n\ts_add_u32 m0, s64, 3072\n\ts_add_u32 s98, s98, 0xd800\n\ts_addc_u32 s99, s99, 0\n\tds_read_b64_tr_b16 v[52:53], v13 offset:10240\n\tv_mfma_f32_32x32x16_bf16 v[92:107], v[72:75], v[232:235], v[92:107]\n\tds_read_b64_tr_b16 v[54:55], v13 offset:47104\n\tglobal_load_lds_dwordx4 v5, s[98:99] offset:0\n\tv_mfma_f32_32x32x16_bf16 v[108:123], v[72:75], v[236:239], v[108:123]\n\tglobal_load_lds_dwordx4 v6, s[98:99] offset:1024\n\tv_mfma_f32_32x32x16_bf16 v[92:107], v[76:79], v[240:243], v[92:107]\n\tglobal_load_lds_dwordx4 v7, s[98:99] offset:2048\n\tv_mfma_f32_32x32x16_bf16 v[108:123], v[76:79], v[244:247], v[108:123]\n\ts_waitcnt vmcnt(20) lgkmcnt(0)\n\ts_barrier\n\ts_mul_i32 s93, s60, 320\n\ts_mul_i32 s92, s61, 8\n\ts_add_u32 s94, s93, s92\n\ts_add_u32 s95, s94, 4160\n\ts_add_u32 s83, s95, 24117248\n\tv_mov_b32 v30, s88\n\tv_mov_b32 v31, s89\n\tbuffer_store_dwordx2 v[30:31], v42, s[56:59], s83 offen sc1\n\tv_mfma_f32_32x32x16_bf16 v[92:107], v[80:83], v[248:251], v[92:107]\n\tds_read_b64_tr_b16 v[56:57], v13 offset:12288\n\ts_add_u32 m0, s64, 6144\n\ts_waitcnt vmcnt(17)\n\tv_dot2_f32_bf16 v20, v204, s96, 0\n\tv_dot2_f32_bf16 v21, v204, s87, 0\n\ts_add_u32 s98, s98, 0xd800\n\ts_addc_u32 s99, s99, 0\n\tv_dot2_f32_bf16 v22, v205, s96, 0\n\tds_read_b64_tr_b16 v[58:59], v13 offset:49152\n\tv_mfma_f32_32x32x16_bf16 v[108:123], v[80:83], v[252:255], v[108:123]\n\tv_dot2_f32_bf16 v23, v205, s87, 0\n\tv_dot2_f32_bf16 v24, v206, s96, 0\n\tds_read_b64_tr_b16 v[60:61], v13 offset:13312\n\tv_dot2_f32_bf16 v25, v206, s87, 0\n\tv_dot2_f32_bf16 v26, v207, s96, 0\n\tv_dot2_f32_bf16 v27, v207, s87, 0\n\tglobal_load_lds_dwordx4 v5, s[98:99] offset:0\n\tv_mfma_f32_32x32x16_bf16 v[92:107], v[84:87], v[156:159], v[92:107]\n\tds_read_b64_tr_b16 v[62:63], v13 offset:50176\n\ts_waitcnt vmcnt(17)\n\tv_dot2_f32_bf16 v20, v208, s96, v20\n\tv_dot2_f32_bf16 v21, v208, s87, v21\n\tv_dot2_f32_bf16 v22, v209, s96, v22\n\tv_dot2_f32_bf16 v23, v209, s87, v23\n\tds_read_b64_tr_b16 v[64:65], v13 offset:14336\n\tv_mfma_f32_32x32x16_bf16 v[108:123], v[84:87], v[160:163], v[108:123]\n\tv_dot2_f32_bf16 v24, v210, s96, v24\n\tv_dot2_f32_bf16 v25, v210, s87, v25\n\tglobal_load_lds_dwordx4 v6, s[98:99] offset:1024\n\tv_dot2_f32_bf16 v26, v211, s96, v26\n\tds_read_b64_tr_b16 v[66:67], v13 offset:51200\n\tv_dot2_f32_bf16 v27, v211, s87, v27\n\tv_mfma_f32_32x32x16_bf16 v[92:107], v[88:91], v[164:167], v[92:107]\n\ts_waitcnt vmcnt(16)\n\tv_dot2_f32_bf16 v20, v212, s96, v20\n\tds_read_b64_tr_b16 v[68:69], v13 offset:15360\n\tv_dot2_f32_bf16 v21, v212, s87, v21\n\tv_dot2_f32_bf16 v22, v213, s96, v22\n\tv_dot2_f32_bf16 v23, v213, s87, v23\n\tv_dot2_f32_bf16 v24, v214, s96, v24\n\tglobal_load_lds_dwordx4 v7, s[98:99] offset:2048\n\tv_mfma_f32_32x32x16_bf16 v[108:123], v[88:91], v[168:171], v[108:123]\n\tds_read_b64_tr_b16 v[70:71], v13 offset:52224\n\tv_dot2_f32_bf16 v25, v214, s87, v25\n\tv_dot2_f32_bf16 v26, v215, s96, v26\n\tv_dot2_f32_bf16 v27, v215, s87, v27\n\tds_read_b64_tr_b16 v[72:73], v13 offset:16384\n\ts_waitcnt vmcnt(15)\n\tv_dot2_f32_bf16 v20, v216, s96, v20\n\tv_mfma_f32_32x32x16_bf16 v[92:107], v[44:47], v[172:175], v[92:107]\n\tv_dot2_f32_bf16 v21, v216, s87, v21\n\tv_dot2_f32_bf16 v22, v217, s96, v22\n\tds_read_b64_tr_b16 v[74:75], v13 offset:53248\n\tv_dot2_f32_bf16 v23, v217, s87, v23\n\tv_dot2_f32_bf16 v24, v218, s96, v24\n\ts_add_u32 m0, s64, 9216\n\tv_dot2_f32_bf16 v25, v218, s87, v25\n\tv_dot2_f32_bf16 v26, v219, s96, v26\n\tv_mfma_f32_32x32x16_bf16 v[108:123], v[44:47], v[176:179], v[108:123]\n\tds_read_b64_tr_b16 v[76:77], v13 offset:17408\n\tds_read_b64_tr_b16 v[78:79], v13 offset:54272\n\ts_add_u32 s98, s98, 0xd800\n\ts_addc_u32 s99, s99, 0\n\tv_dot2_f32_bf16 v27, v219, s87, v27\n\tv_permlane32_swap_b32 v20, v24\n\tv_permlane32_swap_b32 v21, v25\n\tv_permlane32_swap_b32 v22, v26\n\tglobal_load_lds_dwordx4 v5, s[98:99] offset:0\n\tv_mfma_f32_32x32x16_bf16 v[92:107], v[48:51], v[180:183], v[92:107]\n\tv_permlane32_swap_b32 v23, v27\n\tv_add_f32 v20, v20, v24\n\tv_add_f32 v21, v21, v25\n\tv_add_f32 v22, v22, v26\n\tv_add_f32 v23, v23, v27\n\ts_waitcnt vmcnt(15)\n\tv_lshlrev_b32 v24, 16, v28\n\tv_mfma_f32_32x32x16_bf16 v[108:123], v[48:51], v[184:187], v[108:123]\n\tv_and_b32 v25, s77, v28\n\tglobal_load_lds_dwordx4 v6, s[98:99] offset:1024\n\tv_lshlrev_b32 v26, 16, v29\n\tv_and_b32 v27, s77, v29\n\tv_mfma_f32_32x32x16_bf16 v[92:107], v[52:55], v[196:199], v[92:107]\n\tv_fma_f32 v20, v20, s67, v24\n\tv_fma_f32 v21, v21, s67, v25\n\tv_fma_f32 v22, v22, s67, v26\n\tv_fma_f32 v23, v23, s67, v27\n\tv_cvt_pk_bf16_f32 v30, v20, v21\n\tv_cvt_pk_bf16_f32 v31, v22, v23\n\tglobal_load_lds_dwordx4 v7, s[98:99] offset:2048\n\tv_mfma_f32_32x32x16_bf16 v[108:123], v[52:55], v[200:203], v[108:123]\n\ts_add_u32 s83, s71, 0\n\tbuffer_store_dwordx2 v[30:31], v221, s[52:55], s83 offen\n\ts_waitcnt lgkmcnt(0)\n\ts_barrier\n\tv_mfma_f32_32x32x16_bf16 v[124:139], v[56:59], a[0:3], 0\n\tds_read_b64_tr_b16 v[80:81], v14 offset:0\n\ts_add_u32 m0, s64, 12288\n\ts_add_u32 s98, s86, 0x300\n\ts_add_u32 s98, s98, s24\n\ts_addc_u32 s99, s25, 0\n\tds_read_b64_tr_b16 v[82:83], v14 offset:36864\n\tv_mfma_f32_32x32x16_bf16 v[140:155], v[56:59], a[4:7], 0\n\ts_add_u32 s65, s65, 4096\n\tds_read_b64_tr_b16 v[84:85], v14 offset:1024\n\ts_add_u32 s66, s66, 4096\n\tv_mfma_f32_32x32x16_bf16 v[124:139], v[60:63], a[8:11], v[124:139]\n\tds_read_b64_tr_b16 v[86:87], v14 offset:37888\n\tglobal_load_lds_dwordx4 v5, s[98:99] offset:0\n\tv_cvt_pk_bf16_f32 v188, v92, v93\n\tv_mfma_f32_32x32x16_bf16 v[140:155], v[60:63], a[12:15], v[140:155]\n\tds_read_b64_tr_b16 v[88:89], v14 offset:2048\n\tv_cvt_pk_bf16_f32 v189, v94, v95\n\tds_read_b64_tr_b16 v[90:91], v14 offset:38912\n\tv_cvt_pk_bf16_f32 v190, v96, v97\n\tv_mfma_f32_32x32x16_bf16 v[124:139], v[64:67], a[16:19], v[124:139]\n\tds_read_b64_tr_b16 v[44:45], v14 offset:3072\n\tglobal_load_lds_dwordx4 v6, s[98:99] offset:1024\n\tv_cvt_pk_bf16_f32 v191, v98, v99\n\tv_mfma_f32_32x32x16_bf16 v[140:155], v[64:67], a[20:23], v[140:155]\n\tds_read_b64_tr_b16 v[46:47], v14 offset:39936\n\tds_read_b64_tr_b16 v[48:49], v14 offset:4096\n\tglobal_load_lds_dwordx4 v7, s[98:99] offset:2048\n\tv_mfma_f32_32x32x16_bf16 v[124:139], v[68:71], a[24:27], v[124:139]\n\tds_read_b64_tr_b16 v[50:51], v14 offset:40960\n\tds_read_b64_tr_b16 v[52:53], v14 offset:5120\n\tv_mfma_f32_32x32x16_bf16 v[140:155], v[68:71], a[28:31], v[140:155]\n\tds_read_b64_tr_b16 v[54:55], v14 offset:41984\n\ts_add_u32 m0, s64, 15360\n\ts_add_u32 s98, s98, 0xd800\n\ts_addc_u32 s99, s99, 0\n\tbuffer_store_dwordx4 v[188:191], v35, s[56:59], s65 offen offset:0 sc1\n\tv_mfma_f32_32x32x16_bf16 v[124:139], v[72:75], a[32:35], v[124:139]\n\tglobal_load_lds_dwordx4 v5, s[98:99] offset:0\n\tv_mfma_f32_32x32x16_bf16 v[140:155], v[72:75], a[36:39], v[140:155]\n\tv_mfma_f32_32x32x16_bf16 v[124:139], v[76:79], a[40:43], v[124:139]\n\tglobal_load_lds_dwordx4 v6, s[98:99] offset:1024\n\tv_mfma_f32_32x32x16_bf16 v[140:155], v[76:79], a[44:47], v[140:155]\n\ts_waitcnt lgkmcnt(0)\n\ts_barrier\n\tv_mfma_f32_32x32x16_bf16 v[124:139], v[80:83], a[48:51], v[124:139]\n\tds_read_b64_tr_b16 v[56:57], v14 offset:6144\n\tv_mul_u32_u24 v8, 1152, v3\n\tv_add_u32 v8, v8, v4\n\tds_read_b64_tr_b16 v[58:59], v14 offset:43008\n\tv_add_u32 v9, 35840, v8\n\tv_mfma_f32_32x32x16_bf16 v[140:155], v[80:83], a[52:55], v[140:155]\n\tds_read_b64_tr_b16 v[60:61], v14 offset:7168\n\tv_add_u32 v10, 71680, v8\n\ts_add_u32 m0, s64, 73728\n\ts_add_u32 s98, s81, 0x400\n\ts_add_u32 s98, s98, s48\n\ts_addc_u32 s99, s49, 0\n\tv_mfma_f32_32x32x16_bf16 v[124:139], v[84:87], a[56:59], v[124:139]\n\tds_read_b64_tr_b16 v[62:63], v14 offset:44032\n\tv_cvt_pk_bf16_f32 v192, v100, v101\n\tds_read_b64_tr_b16 v[64:65], v14 offset:8192\n\tv_cvt_pk_bf16_f32 v193, v102, v103\n\tv_mfma_f32_32x32x16_bf16 v[140:155], v[84:87], a[60:63], v[140:155]\n\tglobal_load_lds_dwordx4 v8, s[98:99] offset:0\n\tds_read_b64_tr_b16 v[66:67], v14 offset:45056\n\tv_cvt_pk_bf16_f32 v194, v104, v105\n\tv_mfma_f32_32x32x16_bf16 v[124:139], v[88:91], a[64:67], v[124:139]\n\tds_read_b64_tr_b16 v[68:69], v14 offset:9216\n\tv_cvt_pk_bf16_f32 v195, v106, v107\n\tglobal_load_lds_dwordx4 v9, s[98:99] offset:1024\n\tv_mfma_f32_32x32x16_bf16 v[140:155], v[88:91], a[68:71], v[140:155]\n\tds_read_b64_tr_b16 v[70:71], v14 offset:46080\n\tds_read_b64_tr_b16 v[72:73], v14 offset:10240\n\tglobal_load_lds_dwordx4 v10, s[98:99] offset:2048\n\tv_mfma_f32_32x32x16_bf16 v[124:139], v[44:47], a[72:75], v[124:139]\n\tds_read_b64_tr_b16 v[74:75], v14 offset:47104\n\tds_read_b64_tr_b16 v[76:77], v14 offset:11264\n\ts_add_u32 m0, s64, 76800\n\tv_mfma_f32_32x32x16_bf16 v[140:155], v[44:47], a[76:79], v[140:155]\n\tds_read_b64_tr_b16 v[78:79], v14 offset:48128\n\ts_add_u32 s98, s98, 0x1b000\n\ts_addc_u32 s99, s99, 0\n\tbuffer_store_dwordx4 v[192:195], v35, s[56:59], s65 offen offset:256 sc1\n\tv_mfma_f32_32x32x16_bf16 v[124:139], v[48:51], a[80:83], v[124:139]\n\tglobal_load_lds_dwordx4 v8, s[98:99] offset:0\n\tv_mfma_f32_32x32x16_bf16 v[140:155], v[48:51], a[84:87], v[140:155]\n\tglobal_load_lds_dwordx4 v9, s[98:99] offset:1024\n\tv_mfma_f32_32x32x16_bf16 v[124:139], v[52:55], a[88:91], v[124:139]\n\tglobal_load_lds_dwordx4 v10, s[98:99] offset:2048\n\tv_mfma_f32_32x32x16_bf16 v[140:155], v[52:55], a[92:95], v[140:155]\n\ts_waitcnt vmcnt(27) lgkmcnt(0)\n\ts_barrier\n\tv_mfma_f32_32x32x16_bf16 v[124:139], v[56:59], a[96:99], v[124:139]\n\tds_read_b64_tr_b16 v[80:81], v14 offset:12288\n\ts_add_u32 m0, s64, 79872\n\ts_add_u32 s98, s98, 0x1b000\n\ts_addc_u32 s99, s99, 0\n\tds_read_b64_tr_b16 v[82:83], v14 offset:49152\n\tv_mfma_f32_32x32x16_bf16 v[140:155], v[56:59], a[100:103], v[140:155]\n\ts_mul_i32 s94, s60, 320\n\ts_add_u32 s95, s94, 4160\n\ts_add_u32 s78, s95, 24117248\n\tds_read_b64_tr_b16 v[84:85], v14 offset:13312\n\tglobal_load_lds_dwordx4 v8, s[98:99] offset:0\n\tv_mfma_f32_32x32x16_bf16 v[124:139], v[60:63], a[104:107], v[124:139]\n\tv_and_b32 v32, 7, v0\n\tds_read_b64_tr_b16 v[86:87], v14 offset:50176\n\tv_cvt_pk_bf16_f32 v188, v108, v109\n\tv_lshlrev_b32 v32, 3, v32\n\tds_read_b64_tr_b16 v[88:89], v14 offset:14336\n\tv_cvt_pk_bf16_f32 v189, v110, v111\n\tv_mfma_f32_32x32x16_bf16 v[140:155], v[60:63], a[108:111], v[140:155]\n\tbuffer_load_dwordx2 v[222:223], v32, s[56:59], s78 offen sc1\n\tds_read_b64_tr_b16 v[90:91], v14 offset:51200\n\tv_cvt_pk_bf16_f32 v190, v112, v113\n\tv_mfma_f32_32x32x16_bf16 v[124:139], v[64:67], a[112:115], v[124:139]\n\tglobal_load_lds_dwordx4 v9, s[98:99] offset:1024\n\tds_read_b64_tr_b16 v[44:45], v14 offset:15360\n\tv_cvt_pk_bf16_f32 v191, v114, v115\n\tv_mfma_f32_32x32x16_bf16 v[140:155], v[64:67], a[116:119], v[140:155]\n\tds_read_b64_tr_b16 v[46:47], v14 offset:52224\n\tglobal_load_lds_dwordx4 v10, s[98:99] offset:2048\n\tds_read_b64_tr_b16 v[48:49], v14 offset:16384\n\tv_mfma_f32_32x32x16_bf16 v[124:139], v[68:71], a[120:123], v[124:139]\n\tds_read_b64_tr_b16 v[50:51], v14 offset:53248\n\ts_add_u32 m0, s64, 82944\n\ts_add_u32 s98, s98, 0x1b000\n\ts_addc_u32 s99, s99, 0\n\tds_read_b64_tr_b16 v[52:53], v14 offset:17408\n\tv_mfma_f32_32x32x16_bf16 v[140:155], v[68:71], a[124:127], v[140:155]\n\tds_read_b64_tr_b16 v[54:55], v14 offset:54272\n\tglobal_load_lds_dwordx4 v8, s[98:99] offset:0\n\tv_mfma_f32_32x32x16_bf16 v[124:139], v[72:75], a[128:131], v[124:139]\n\tglobal_load_lds_dwordx4 v9, s[98:99] offset:1024\n\tv_mfma_f32_32x32x16_bf16 v[140:155], v[72:75], a[132:135], v[140:155]\n\tbuffer_store_dwordx4 v[188:191], v35, s[56:59], s66 offen offset:0 sc1\n\tv_mfma_f32_32x32x16_bf16 v[124:139], v[76:79], a[136:139], v[124:139]\n\tglobal_load_lds_dwordx4 v10, s[98:99] offset:2048\n\tv_mfma_f32_32x32x16_bf16 v[140:155], v[76:79], a[140:143], v[140:155]\n\ts_waitcnt vmcnt(29) lgkmcnt(0)\n\ts_barrier\n\tv_mfma_f32_32x32x16_bf16 v[124:139], v[80:83], a[144:147], v[124:139]\n\tds_read_b64_tr_b16 v[56:57], v13 offset:0\n\ts_add_u32 m0, s64, 86016\n\ts_add_u32 s98, s98, 0x1b000\n\ts_addc_u32 s99, s99, 0\n\tds_read_b64_tr_b16 v[58:59], v13 offset:36864\n\tv_mfma_f32_32x32x16_bf16 v[140:155], v[80:83], a[148:151], v[140:155]\n\tds_read_b64_tr_b16 v[60:61], v13 offset:1024\n\tglobal_load_lds_dwordx4 v8, s[98:99] offset:0\n\tv_mfma_f32_32x32x16_bf16 v[124:139], v[84:87], a[152:155], v[124:139]\n\tds_read_b64_tr_b16 v[62:63], v13 offset:37888\n\tv_cvt_pk_bf16_f32 v192, v116, v117\n\tds_read_b64_tr_b16 v[64:65], v13 offset:2048\n\tv_cvt_pk_bf16_f32 v193, v118, v119\n\tv_mfma_f32_32x32x16_bf16 v[140:155], v[84:87], a[156:159], v[140:155]\n\tglobal_load_lds_dwordx4 v9, s[98:99] offset:1024\n\tds_read_b64_tr_b16 v[66:67], v13 offset:38912\n\tv_cvt_pk_bf16_f32 v194, v120, v121\n\tv_mfma_f32_32x32x16_bf16 v[124:139], v[88:91], a[160:163], v[124:139]\n\tds_read_b64_tr_b16 v[68:69], v13 offset:3072\n\tv_cvt_pk_bf16_f32 v195, v122, v123\n\tglobal_load_lds_dwordx4 v10, s[98:99] offset:2048\n\tv_mfma_f32_32x32x16_bf16 v[140:155], v[88:91], a[164:167], v[140:155]\n\tds_read_b64_tr_b16 v[70:71], v13 offset:39936\n\tds_read_b64_tr_b16 v[72:73], v13 offset:4096\n\tbuffer_store_dwordx4 v[192:195], v35, s[56:59], s66 offen offset:256 sc1\n\tv_mfma_f32_32x32x16_bf16 v[124:139], v[44:47], a[168:171], v[124:139]\n\tds_read_b64_tr_b16 v[74:75], v13 offset:40960\n\ts_add_u32 m0, s64, 89088\n\ts_add_u32 s98, s98, 0x1b000\n\ts_addc_u32 s99, s99, 0\n\tds_read_b64_tr_b16 v[76:77], v13 offset:5120\n\tv_mfma_f32_32x32x16_bf16 v[140:155], v[44:47], a[172:175], v[140:155]\n\tds_read_b64_tr_b16 v[78:79], v13 offset:41984\n\tglobal_load_lds_dwordx4 v8, s[98:99] offset:0\n\tv_mfma_f32_32x32x16_bf16 v[124:139], v[48:51], a[176:179], v[124:139]\n\tglobal_load_lds_dwordx4 v9, s[98:99] offset:1024\n\tv_mfma_f32_32x32x16_bf16 v[140:155], v[48:51], a[180:183], v[140:155]\n\tv_mfma_f32_32x32x16_bf16 v[124:139], v[52:55], a[184:187], v[124:139]\n\tglobal_load_lds_dwordx4 v10, s[98:99] offset:2048\n\tv_mfma_f32_32x32x16_bf16 v[140:155], v[52:55], a[188:191], v[140:155]\n\ts_waitcnt vmcnt(29) lgkmcnt(0)\n\ts_barrier\n\ts_waitcnt vmcnt(13)\n\tv_cmp_ne_u64 vcc, s[88:89], v[222:223]\n\ts_nop 3\n\ts_cbranch_vccz L_ok_r1_%=\n\ts_mov_b32 s83, 0\n\tL_sp_r1_%=:\n\ts_sleep 1\n\tbuffer_load_dwordx2 v[222:223], v32, s[56:59], s78 offen sc1\n\ts_waitcnt vmcnt(0)\n\tv_cmp_ne_u64 vcc, s[88:89], v[222:223]\n\ts_add_u32 s83, s83, 1\n\ts_nop 2\n\ts_cbranch_vccz L_ok_r1_%=\n\ts_cmp_lt_u32 s83, 16384\n\ts_cbranch_scc1 L_sp_r1_%=\n\tL_ok_r1_%=:\n\tv_mfma_f32_32x32x16_bf16 v[124:139], v[56:59], a[192:195], v[124:139]\n\ts_add_u32 s83, s70, 2048\n\tbuffer_load_dwordx4 v[204:207], v220, s[56:59], s83 offen sc1\n\tds_read_b64_tr_b16 v[80:81], v13 offset:6144\n\tv_mfma_f32_32x32x16_bf16 v[140:155], v[56:59], a[196:199], v[140:155]\n\tds_read_b64_tr_b16 v[82:83], v13 offset:43008\n\ts_add_u32 m0, s64, 0\n\ts_add_u32 s98, s86, 0x400\n\ts_add_u32 s98, s98, s24\n\ts_addc_u32 s99, s25, 0\n\tds_read_b64_tr_b16 v[84:85], v13 offset:7168\n\tv_mfma_f32_32x32x16_bf16 v[124:139], v[60:63], a[200:203], v[124:139]\n\ts_add_u32 s83, s70, 20480\n\tbuffer_load_dwordx4 v[208:211], v220, s[56:59], s83 offen sc1\n\tds_read_b64_tr_b16 v[86:87], v13 offset:44032\n\tv_mfma_f32_32x32x16_bf16 v[140:155], v[60:63], a[204:207], v[140:155]\n\ts_add_u32 s83, s70, 38912\n\tbuffer_load_dwordx4 v[212:215], v220, s[56:59], s83 offen sc1\n\tds_read_b64_tr_b16 v[88:89], v13 offset:8192\n\tv_mfma_f32_32x32x16_bf16 v[124:139], v[64:67], a[208:211], v[124:139]\n\tds_read_b64_tr_b16 v[90:91], v13 offset:45056\n\tglobal_load_lds_dwordx4 v8, s[98:99] offset:0\n\tds_read_b64_tr_b16 v[44:45], v13 offset:9216\n\tv_mfma_f32_32x32x16_bf16 v[140:155], v[64:67], a[212:215], v[140:155]\n\ts_add_u32 s83, s70, 57344\n\tbuffer_load_dwordx4 v[216:219], v220, s[56:59], s83 offen sc1\n\tds_read_b64_tr_b16 v[46:47], v13 offset:46080\n\tv_mfma_f32_32x32x16_bf16 v[124:139], v[68:71], a[216:219], v[124:139]\n\ts_add_u32 s83, s71, 256\n\tbuffer_load_dwordx2 v[28:29], v221, s[40:43], s83 offen\n\tds_read_b64_tr_b16 v[48:49], v13 offset:10240\n\tv_mfma_f32_32x32x16_bf16 v[140:155], v[68:71], a[220:223], v[140:155]\n\tds_read_b64_tr_b16 v[50:51], v13 offset:47104\n\tds_read_b64_tr_b16 v[52:53], v13 offset:11264\n\tds_read_b64_tr_b16 v[54:55], v13 offset:48128\n\tv_mfma_f32_32x32x16_bf16 v[124:139], v[72:75], a[224:227], v[124:139]\n\tglobal_load_lds_dwordx4 v9, s[98:99] offset:1024\n\tv_mfma_f32_32x32x16_bf16 v[140:155], v[72:75], a[228:231], v[140:155]\n\tglobal_load_lds_dwordx4 v10, s[98:99] offset:2048\n\tv_mfma_f32_32x32x16_bf16 v[124:139], v[76:79], a[232:235], v[124:139]\n\tv_mfma_f32_32x32x16_bf16 v[140:155], v[76:79], a[236:239], v[140:155]\n\ts_waitcnt lgkmcnt(0)\n\ts_barrier\n\tv_mfma_f32_32x32x16_bf16 v[124:139], v[80:83], a[240:243], v[124:139]\n\tds_read_b64_tr_b16 v[56:57], v13 offset:12288\n\tds_read_b64_tr_b16 v[58:59], v13 offset:49152\n\tv_mfma_f32_32x32x16_bf16 v[140:155], v[80:83], a[244:247], v[140:155]\n\tds_read_b64_tr_b16 v[60:61], v13 offset:13312\n\tv_mfma_f32_32x32x16_bf16 v[124:139], v[84:87], a[248:251], v[124:139]\n\tds_read_b64_tr_b16 v[62:63], v13 offset:50176\n\tds_read_b64_tr_b16 v[64:65], v13 offset:14336\n\tv_mfma_f32_32x32x16_bf16 v[140:155], v[84:87], a[252:255], v[140:155]\n\tds_read_b64_tr_b16 v[66:67], v13 offset:51200\n\tv_mfma_f32_32x32x16_bf16 v[124:139], v[88:91], v[224:227], v[124:139]\n\tds_read_b64_tr_b16 v[68:69], v13 offset:15360\n\tds_read_b64_tr_b16 v[70:71], v13 offset:52224\n\tv_mfma_f32_32x32x16_bf16 v[140:155], v[88:91], v[228:231], v[140:155]\n\tds_read_b64_tr_b16 v[72:73], v13 offset:16384\n\tv_mfma_f32_32x32x16_bf16 v[124:139], v[44:47], v[232:235], v[124:139]\n\tds_read_b64_tr_b16 v[74:75], v13 offset:53248\n\tv_mfma_f32_32x32x16_bf16 v[140:155], v[44:47], v[236:239], v[140:155]\n\tv_mfma_f32_32x32x16_bf16 v[124:139], v[48:51], v[240:243], v[124:139]\n\tv_mfma_f32_32x32x16_bf16 v[140:155], v[48:51], v[244:247], v[140:155]\n\ts_waitcnt vmcnt(11) lgkmcnt(0)\n\ts_barrier\n\ts_mul_i32 s93, s60, 320\n\ts_mul_i32 s92, s61, 8\n\ts_add_u32 s94, s93, s92\n\ts_add_u32 s95, s94, 4224\n\ts_add_u32 s83, s95, 24117248\n\tv_mov_b32 v30, s88\n\tv_mov_b32 v31, s89\n\tbuffer_store_dwordx2 v[30:31], v42, s[56:59], s83 offen sc1\n\tv_mfma_f32_32x32x16_bf16 v[124:139], v[52:55], v[248:251], v[124:139]\n\tv_add_u32 v15, 73728, v15\n\tv_add_u32 v16, 73728, v16\n\ts_waitcnt vmcnt(8)\n\tv_dot2_f32_bf16 v20, v204, s96, 0\n\tv_dot2_f32_bf16 v21, v204, s87, 0\n\tv_dot2_f32_bf16 v22, v205, s96, 0\n\tv_dot2_f32_bf16 v23, v205, s87, 0\n\tv_mfma_f32_32x32x16_bf16 v[140:155], v[52:55], v[252:255], v[140:155]\n\tds_read_b64_tr_b16 v[76:77], v15 offset:0\n\tds_read_b64_tr_b16 v[78:79], v15 offset:18432\n\tds_read_b64_tr_b16 v[80:81], v16 offset:0\n\tv_dot2_f32_bf16 v24, v206, s96, 0\n\tv_dot2_f32_bf16 v25, v206, s87, 0\n\tv_dot2_f32_bf16 v26, v207, s96, 0\n\tv_mfma_f32_32x32x16_bf16 v[124:139], v[56:59], v[156:159], v[124:139]\n\tds_read_b64_tr_b16 v[82:83], v16 offset:18432\n\tds_read_b64_tr_b16 v[84:85], v15 offset:1024\n\tv_dot2_f32_bf16 v27, v207, s87, 0\n\ts_waitcnt vmcnt(7)\n\tv_dot2_f32_bf16 v20, v208, s96, v20\n\tv_dot2_f32_bf16 v21, v208, s87, v21\n\tv_mfma_f32_32x32x16_bf16 v[140:155], v[56:59], v[160:163], v[140:155]\n\tds_read_b64_tr_b16 v[86:87], v15 offset:19456\n\tds_read_b64_tr_b16 v[88:89], v16 offset:1024\n\tv_dot2_f32_bf16 v22, v209, s96, v22\n\tv_dot2_f32_bf16 v23, v209, s87, v23\n\tv_dot2_f32_bf16 v24, v210, s96, v24\n\tv_mfma_f32_32x32x16_bf16 v[124:139], v[60:63], v[164:167], v[124:139]\n\tds_read_b64_tr_b16 v[90:91], v16 offset:19456\n\tv_dot2_f32_bf16 v25, v210, s87, v25\n\tv_dot2_f32_bf16 v26, v211, s96, v26\n\tv_dot2_f32_bf16 v27, v211, s87, v27\n\tv_mfma_f32_32x32x16_bf16 v[140:155], v[60:63], v[168:171], v[140:155]\n\tds_read_b64_tr_b16 v[44:45], v15 offset:2048\n\ts_waitcnt vmcnt(6)\n\tv_dot2_f32_bf16 v20, v212, s96, v20\n\tv_dot2_f32_bf16 v21, v212, s87, v21\n\tv_dot2_f32_bf16 v22, v213, s96, v22\n\tv_mfma_f32_32x32x16_bf16 v[124:139], v[64:67], v[172:175], v[124:139]\n\tds_read_b64_tr_b16 v[46:47], v15 offset:20480\n\tds_read_b64_tr_b16 v[48:49], v16 offset:2048\n\tv_dot2_f32_bf16 v23, v213, s87, v23\n\tv_dot2_f32_bf16 v24, v214, s96, v24\n\tv_dot2_f32_bf16 v25, v214, s87, v25\n\tv_dot2_f32_bf16 v26, v215, s96, v26\n\tv_mfma_f32_32x32x16_bf16 v[140:155], v[64:67], v[176:179], v[140:155]\n\tds_read_b64_tr_b16 v[50:51], v16 offset:20480\n\tds_read_b64_tr_b16 v[52:53], v15 offset:3072\n\tv_dot2_f32_bf16 v27, v215, s87, v27\n\ts_waitcnt vmcnt(4)\n\tv_dot2_f32_bf16 v20, v216, s96, v20\n\tv_dot2_f32_bf16 v21, v216, s87, v21\n\tv_mfma_f32_32x32x16_bf16 v[124:139], v[68:71], v[180:183], v[124:139]\n\tds_read_b64_tr_b16 v[54:55], v15 offset:21504\n\tv_dot2_f32_bf16 v22, v217, s96, v22\n\tv_dot2_f32_bf16 v23, v217, s87, v23\n\tv_dot2_f32_bf16 v24, v218, s96, v24\n\tv_mfma_f32_32x32x16_bf16 v[140:155], v[68:71], v[184:187], v[140:155]\n\tds_read_b64_tr_b16 v[56:57], v16 offset:3072\n\tv_dot2_f32_bf16 v25, v218, s87, v25\n\tv_dot2_f32_bf16 v26, v219, s96, v26\n\tv_dot2_f32_bf16 v27, v219, s87, v27\n\tv_mfma_f32_32x32x16_bf16 v[124:139], v[72:75], v[196:199], v[124:139]\n\tds_read_b64_tr_b16 v[58:59], v16 offset:21504\n\tds_read_b64_tr_b16 v[60:61], v15 offset:4096\n\tv_permlane32_swap_b32 v20, v24\n\tv_permlane32_swap_b32 v21, v25\n\tv_permlane32_swap_b32 v22, v26\n\tv_mfma_f32_32x32x16_bf16 v[140:155], v[72:75], v[200:203], v[140:155]\n\tds_read_b64_tr_b16 v[62:63], v15 offset:22528\n\tds_read_b64_tr_b16 v[64:65], v16 offset:4096\n\tv_permlane32_swap_b32 v23, v27\n\tv_add_f32 v20, v20, v24\n\tv_add_f32 v21, v21, v25\n\ts_waitcnt lgkmcnt(15)\n\tv_mfma_f32_32x32x16_bf16 v[92:107], v[76:79], a[0:3], 0\n\tds_read_b64_tr_b16 v[66:67], v16 offset:22528\n\tds_read_b64_tr_b16 v[68:69], v15 offset:5120\n\tv_add_f32 v22, v22, v26\n\tv_add_f32 v23, v23, v27\n\ts_waitcnt vmcnt(3)\n\tv_lshlrev_b32 v24, 16, v28\n\tv_and_b32 v25, s77, v28\n\tv_mfma_f32_32x32x16_bf16 v[92:107], v[80:83], a[8:11], v[92:107]\n\tds_read_b64_tr_b16 v[70:71], v15 offset:23552\n\tv_cvt_pk_bf16_f32 v188, v124, v125\n\tv_cvt_pk_bf16_f32 v189, v126, v127\n\tv_cvt_pk_bf16_f32 v190, v128, v129\n\tv_lshlrev_b32 v26, 16, v29\n\tv_and_b32 v27, s77, v29\n\tv_cvt_pk_bf16_f32 v191, v130, v131\n\tv_fma_f32 v20, v20, s67, v24\n\ts_waitcnt lgkmcnt(15)\n\tv_mfma_f32_32x32x16_bf16 v[92:107], v[84:87], a[16:19], v[92:107]\n\tds_read_b64_tr_b16 v[72:73], v16 offset:5120\n\tv_fma_f32 v21, v21, s67, v25\n\tv_fma_f32 v22, v22, s67, v26\n\tv_fma_f32 v23, v23, s67, v27\n\tbuffer_store_dwordx4 v[188:191], v35, s[56:59], s65 offen offset:2048 sc1\n\ts_waitcnt lgkmcnt(15)\n\tv_mfma_f32_32x32x16_bf16 v[92:107], v[88:91], a[24:27], v[92:107]\n\tds_read_b64_tr_b16 v[74:75], v16 offset:23552\n\tv_cvt_pk_bf16_f32 v30, v20, v21\n\tv_cvt_pk_bf16_f32 v31, v22, v23\n\ts_waitcnt lgkmcnt(14)\n\tv_mfma_f32_32x32x16_bf16 v[92:107], v[44:47], a[32:35], v[92:107]\n\ts_add_u32 s83, s71, 256\n\tbuffer_store_dwordx2 v[30:31], v221, s[52:55], s83 offen\n\ts_waitcnt lgkmcnt(12)\n\tv_mfma_f32_32x32x16_bf16 v[92:107], v[48:51], a[40:43], v[92:107]\n\ts_waitcnt lgkmcnt(0)\n\ts_barrier\n\tv_mfma_f32_32x32x16_bf16 v[92:107], v[52:55], a[48:51], v[92:107]\n\tds_read_b64_tr_b16 v[76:77], v15 offset:6144\n\tds_read_b64_tr_b16 v[80:81], v16 offset:6144\n\tv_mfma_f32_32x32x16_bf16 v[92:107], v[56:59], a[56:59], v[92:107]\n\tds_read_b64_tr_b16 v[78:79], v15 offset:24576\n\tds_read_b64_tr_b16 v[82:83], v16 offset:24576\n\tds_read_b64_tr_b16 v[84:85], v15 offset:7168\n\tv_mfma_f32_32x32x16_bf16 v[92:107], v[60:63], a[64:67], v[92:107]\n\tds_read_b64_tr_b16 v[86:87], v15 offset:25600\n\tds_read_b64_tr_b16 v[88:89], v16 offset:7168\n\tv_cvt_pk_bf16_f32 v192, v132, v133\n\tv_cvt_pk_bf16_f32 v193, v134, v135\n\tv_mfma_f32_32x32x16_bf16 v[92:107], v[64:67], a[72:75], v[92:107]\n\tds_read_b64_tr_b16 v[90:91], v16 offset:25600\n\tds_read_b64_tr_b16 v[44:45], v15 offset:8192\n\tv_cvt_pk_bf16_f32 v194, v136, v137\n\tv_cvt_pk_bf16_f32 v195, v138, v139\n\tv_mfma_f32_32x32x16_bf16 v[92:107], v[68:71], a[80:83], v[92:107]\n\tds_read_b64_tr_b16 v[46:47], v15 offset:26624\n\tds_read_b64_tr_b16 v[48:49], v16 offset:8192\n\tbuffer_store_dwordx4 v[192:195], v35, s[56:59], s65 offen offset:2304 sc1\n\tv_mfma_f32_32x32x16_bf16 v[92:107], v[72:75], a[88:91], v[92:107]\n\tds_read_b64_tr_b16 v[50:51], v16 offset:26624\n\tds_read_b64_tr_b16 v[52:53], v15 offset:9216\n\ts_waitcnt lgkmcnt(10)\n\tv_mfma_f32_32x32x16_bf16 v[92:107], v[76:79], a[96:99], v[92:107]\n\tds_read_b64_tr_b16 v[54:55], v15 offset:27648\n\tds_read_b64_tr_b16 v[56:57], v16 offset:9216\n\tds_read_b64_tr_b16 v[60:61], v15 offset:10240\n\ts_waitcnt lgkmcnt(12)\n\tv_mfma_f32_32x32x16_bf16 v[92:107], v[80:83], a[104:107], v[92:107]\n\tds_read_b64_tr_b16 v[58:59], v16 offset:27648\n\tds_read_b64_tr_b16 v[62:63], v15 offset:28672\n\tds_read_b64_tr_b16 v[64:65], v16 offset:10240\n\ts_waitcnt lgkmcnt(13)\n\tv_mfma_f32_32x32x16_bf16 v[92:107], v[84:87], a[112:115], v[92:107]\n\tds_read_b64_tr_b16 v[66:67], v16 offset:28672\n\tds_read_b64_tr_b16 v[68:69], v15 offset:11264\n\ts_waitcnt lgkmcnt(13)\n\tv_mfma_f32_32x32x16_bf16 v[92:107], v[88:91], a[120:123], v[92:107]\n\tds_read_b64_tr_b16 v[70:71], v15 offset:29696\n\tds_read_b64_tr_b16 v[72:73], v16 offset:11264\n\ts_waitcnt lgkmcnt(13)\n\tv_mfma_f32_32x32x16_bf16 v[92:107], v[44:47], a[128:131], v[92:107]\n\tds_read_b64_tr_b16 v[74:75], v16 offset:29696\n\ts_waitcnt lgkmcnt(12)\n\tv_mfma_f32_32x32x16_bf16 v[92:107], v[48:51], a[136:139], v[92:107]\n\ts_waitcnt lgkmcnt(0)\n\ts_barrier\n\tv_mfma_f32_32x32x16_bf16 v[92:107], v[52:55], a[144:147], v[92:107]\n\tds_read_b64_tr_b16 v[76:77], v15 offset:12288\n\tds_read_b64_tr_b16 v[80:81], v16 offset:12288\n\tv_mfma_f32_32x32x16_bf16 v[92:107], v[56:59], a[152:155], v[92:107]\n\tds_read_b64_tr_b16 v[78:79], v15 offset:30720\n\tds_read_b64_tr_b16 v[82:83], v16 offset:30720\n\tds_read_b64_tr_b16 v[84:85], v15 offset:13312\n\tv_mfma_f32_32x32x16_bf16 v[92:107], v[60:63], a[160:163], v[92:107]\n\tds_read_b64_tr_b16 v[86:87], v15 offset:31744\n\tds_read_b64_tr_b16 v[88:89], v16 offset:13312\n\tv_cvt_pk_bf16_f32 v188, v140, v141\n\tv_cvt_pk_bf16_f32 v189, v142, v143\n\tv_mfma_f32_32x32x16_bf16 v[92:107], v[64:67], a[168:171], v[92:107]\n\tds_read_b64_tr_b16 v[90:91], v16 offset:31744\n\tds_read_b64_tr_b16 v[44:45], v15 offset:14336\n\tv_cvt_pk_bf16_f32 v190, v144, v145\n\tv_cvt_pk_bf16_f32 v191, v146, v147\n\tv_mfma_f32_32x32x16_bf16 v[92:107], v[68:71], a[176:179], v[92:107]\n\tds_read_b64_tr_b16 v[46:47], v15 offset:32768\n\tds_read_b64_tr_b16 v[48:49], v16 offset:14336\n\tbuffer_store_dwordx4 v[188:191], v35, s[56:59], s66 offen offset:2048 sc1\n\tv_mfma_f32_32x32x16_bf16 v[92:107], v[72:75], a[184:187], v[92:107]\n\tds_read_b64_tr_b16 v[50:51], v16 offset:32768\n\tds_read_b64_tr_b16 v[52:53], v15 offset:15360\n\ts_waitcnt lgkmcnt(10)\n\tv_mfma_f32_32x32x16_bf16 v[92:107], v[76:79], a[192:195], v[92:107]\n\tds_read_b64_tr_b16 v[54:55], v15 offset:33792\n\tds_read_b64_tr_b16 v[56:57], v16 offset:15360\n\tds_read_b64_tr_b16 v[60:61], v15 offset:16384\n\ts_waitcnt lgkmcnt(12)\n\tv_mfma_f32_32x32x16_bf16 v[92:107], v[80:83], a[200:203], v[92:107]\n\tds_read_b64_tr_b16 v[58:59], v16 offset:33792\n\tds_read_b64_tr_b16 v[62:63], v15 offset:34816\n\tds_read_b64_tr_b16 v[64:65], v16 offset:16384\n\ts_waitcnt lgkmcnt(13)\n\tv_mfma_f32_32x32x16_bf16 v[92:107], v[84:87], a[208:211], v[92:107]\n\tds_read_b64_tr_b16 v[66:67], v16 offset:34816\n\tds_read_b64_tr_b16 v[68:69], v15 offset:17408\n\ts_waitcnt lgkmcnt(13)\n\tv_mfma_f32_32x32x16_bf16 v[92:107], v[88:91], a[216:219], v[92:107]\n\tds_read_b64_tr_b16 v[70:71], v15 offset:35840\n\tds_read_b64_tr_b16 v[72:73], v16 offset:17408\n\ts_waitcnt lgkmcnt(13)\n\tv_mfma_f32_32x32x16_bf16 v[92:107], v[44:47], a[224:227], v[92:107]\n\tds_read_b64_tr_b16 v[74:75], v16 offset:35840\n\ts_waitcnt lgkmcnt(12)\n\tv_mfma_f32_32x32x16_bf16 v[92:107], v[48:51], a[232:235], v[92:107]\n\ts_waitcnt vmcnt(5) lgkmcnt(0)\n\ts_barrier\n\tv_mfma_f32_32x32x16_bf16 v[92:107], v[52:55], a[240:243], v[92:107]\n\tv_subrev_u32 v15, 73728, v15\n\tv_subrev_u32 v16, 73728, v16\n\tds_read_b64_tr_b16 v[76:77], v15 offset:0\n\tv_mfma_f32_32x32x16_bf16 v[92:107], v[56:59], a[248:251], v[92:107]\n\tds_read_b64_tr_b16 v[78:79], v15 offset:18432\n\tds_read_b64_tr_b16 v[80:81], v16 offset:0\n\tds_read_b64_tr_b16 v[82:83], v16 offset:18432\n\tv_mfma_f32_32x32x16_bf16 v[92:107], v[60:63], v[224:227], v[92:107]\n\tv_cvt_pk_bf16_f32 v192, v148, v149\n\tv_cvt_pk_bf16_f32 v193, v150, v151\n\tds_read_b64_tr_b16 v[84:85], v15 offset:1024\n\tv_cvt_pk_bf16_f32 v194, v152, v153\n\tds_read_b64_tr_b16 v[86:87], v15 offset:19456\n\tv_cvt_pk_bf16_f32 v195, v154, v155\n\tds_read_b64_tr_b16 v[88:89], v16 offset:1024\n\tbuffer_store_dwordx4 v[192:195], v35, s[56:59], s66 offen offset:2304 sc1\n\tv_mfma_f32_32x32x16_bf16 v[92:107], v[64:67], v[232:235], v[92:107]\n\tds_read_b64_tr_b16 v[90:91], v16 offset:19456\n\tds_read_b64_tr_b16 v[44:45], v15 offset:2048\n\tds_read_b64_tr_b16 v[46:47], v15 offset:20480\n\tv_mfma_f32_32x32x16_bf16 v[92:107], v[68:71], v[240:243], v[92:107]\n\ts_waitcnt lgkmcnt(0)\n\ts_barrier\n\tv_mfma_f32_32x32x16_bf16 v[92:107], v[72:75], v[248:251], v[92:107]\n\tv_mfma_f32_32x32x16_bf16 v[92:107], v[76:79], v[156:159], v[92:107]\n\tv_mfma_f32_32x32x16_bf16 v[92:107], v[80:83], v[164:167], v[92:107]\n\tv_mfma_f32_32x32x16_bf16 v[92:107], v[84:87], v[172:175], v[92:107]\n\tv_mfma_f32_32x32x16_bf16 v[92:107], v[88:91], v[180:183], v[92:107]\n\tv_mfma_f32_32x32x16_bf16 v[92:107], v[44:47], v[196:199], v[92:107]\n\ts_nop 7\n\ts_nop 7\n\tv_cvt_pk_bf16_f32 v188, v92, v93\n\tv_cvt_pk_bf16_f32 v189, v94, v95\n\tv_cvt_pk_bf16_f32 v190, v96, v97\n\tv_cvt_pk_bf16_f32 v191, v98, v99\n\tbuffer_store_dwordx4 v[188:191], v36, s[56:59], s65 offen offset:0 sc1\n\tv_cvt_pk_bf16_f32 v192, v100, v101\n\tv_cvt_pk_bf16_f32 v193, v102, v103\n\tv_cvt_pk_bf16_f32 v194, v104, v105\n\tv_cvt_pk_bf16_f32 v195, v106, v107\n\tbuffer_store_dwordx4 v[192:195], v36, s[56:59], s65 offen offset:256 sc1\n\ts_waitcnt vmcnt(0)\n\ts_barrier\n\ts_mul_i32 s93, s60, 320\n\ts_mul_i32 s92, s61, 8\n\ts_add_u32 s94, s93, s92\n\ts_add_u32 s95, s94, 4288\n\ts_add_u32 s83, s95, 24117248\n\tv_mov_b32 v30, s88\n\tv_mov_b32 v31, s89\n\tbuffer_store_dwordx2 v[30:31], v42, s[56:59], s83 offen sc1\n\ts_mul_i32 s93, s60, 320\n\ts_mul_i32 s92, s61, 8\n\ts_add_u32 s94, s93, s92\n\ts_add_u32 s95, s94, 4352\n\ts_add_u32 s83, s95, 24117248\n\tv_mov_b32 v30, s88\n\tv_mov_b32 v31, s89\n\tbuffer_store_dwordx2 v[30:31], v42, s[56:59], s83 offen sc1\n\ts_mul_i32 s95, s60, 320\n\ts_add_u32 s78, s95, 24121472\n\tv_min_u32 v32, 23, v0\n\tv_lshlrev_b32 v32, 3, v32\n\tbuffer_load_dwordx2 v[222:223], v32, s[56:59], s78 offen sc1\n\ts_waitcnt vmcnt(0)\n\tv_cmp_ne_u64 vcc, s[88:89], v[222:223]\n\ts_nop 3\n\ts_cbranch_vccz L_ok_tail_%=\n\ts_mov_b32 s83, 0\n\tL_sp_tail_%=:\n\ts_sleep 1\n\tbuffer_load_dwordx2 v[222:223], v32, s[56:59], s78 offen sc1\n\ts_waitcnt vmcnt(0)\n\tv_cmp_ne_u64 vcc, s[88:89], v[222:223]\n\ts_add_u32 s83, s83, 1\n\ts_nop 2\n\ts_cbranch_vccz L_ok_tail_%=\n\ts_cmp_lt_u32 s83, 16384\n\ts_cbranch_scc1 L_sp_tail_%=\n\tL_ok_tail_%=:\n\ts_add_u32 s83, s70, 4096\n\tbuffer_load_dwordx4 v[44:47], v220, s[56:59], s83 offen sc1\n\ts_add_u32 s83, s70, 22528\n\tbuffer_load_dwordx4 v[48:51], v220, s[56:59], s83 offen sc1\n\ts_add_u32 s83, s70, 40960\n\tbuffer_load_dwordx4 v[52:55], v220, s[56:59], s83 offen sc1\n\ts_add_u32 s83, s70, 59392\n\tbuffer_load_dwordx4 v[56:59], v220, s[56:59], s83 offen sc1\n\ts_add_u32 s83, s71, 512\n\tbuffer_load_dwordx2 v[60:61], v221, s[40:43], s83 offen\n\ts_add_u32 s83, s70, 6144\n\tbuffer_load_dwordx4 v[64:67], v220, s[56:59], s83 offen sc1\n\ts_add_u32 s83, s70, 24576\n\tbuffer_load_dwordx4 v[68:71], v220, s[56:59], s83 offen sc1\n\ts_add_u32 s83, s70, 43008\n\tbuffer_load_dwordx4 v[72:75], v220, s[56:59], s83 offen sc1\n\ts_add_u32 s83, s70, 61440\n\tbuffer_load_dwordx4 v[76:79], v220, s[56:59], s83 offen sc1\n\ts_add_u32 s83, s71, 768\n\tbuffer_load_dwordx2 v[80:81], v221, s[40:43], s83 offen\n\ts_add_u32 s83, s70, 0\n\tbuffer_load_dwordx4 v[204:207], v33, s[56:59], s83 offen sc1\n\ts_add_u32 s83, s70, 36864\n\tbuffer_load_dwordx4 v[208:211], v33, s[56:59], s83 offen sc1\n\ts_add_u32 s83, s71, 0\n\tbuffer_load_dwordx2 v[28:29], v34, s[40:43], s83 offen\n\ts_waitcnt vmcnt(12)\n\tv_dot2_f32_bf16 v20, v44, s96, 0\n\tv_dot2_f32_bf16 v21, v44, s87, 0\n\tv_dot2_f32_bf16 v22, v45, s96, 0\n\tv_dot2_f32_bf16 v23, v45, s87, 0\n\tv_dot2_f32_bf16 v24, v46, s96, 0\n\tv_dot2_f32_bf16 v25, v46, s87, 0\n\tv_dot2_f32_bf16 v26, v47, s96, 0\n\tv_dot2_f32_bf16 v27, v47, s87, 0\n\ts_waitcnt vmcnt(11)\n\tv_dot2_f32_bf16 v20, v48, s96, v20\n\tv_dot2_f32_bf16 v21, v48, s87, v21\n\tv_dot2_f32_bf16 v22, v49, s96, v22\n\tv_dot2_f32_bf16 v23, v49, s87, v23\n\tv_dot2_f32_bf16 v24, v50, s96, v24\n\tv_dot2_f32_bf16 v25, v50, s87, v25\n\tv_dot2_f32_bf16 v26, v51, s96, v26\n\tv_dot2_f32_bf16 v27, v51, s87, v27\n\ts_waitcnt vmcnt(10)\n\tv_dot2_f32_bf16 v20, v52, s96, v20\n\tv_dot2_f32_bf16 v21, v52, s87, v21\n\tv_dot2_f32_bf16 v22, v53, s96, v22\n\tv_dot2_f32_bf16 v23, v53, s87, v23\n\tv_dot2_f32_bf16 v24, v54, s96, v24\n\tv_dot2_f32_bf16 v25, v54, s87, v25\n\tv_dot2_f32_bf16 v26, v55, s96, v26\n\tv_dot2_f32_bf16 v27, v55, s87, v27\n\ts_waitcnt vmcnt(9)\n\tv_dot2_f32_bf16 v20, v56, s96, v20\n\tv_dot2_f32_bf16 v21, v56, s87, v21\n\tv_dot2_f32_bf16 v22, v57, s96, v22\n\tv_dot2_f32_bf16 v23, v57, s87, v23\n\tv_dot2_f32_bf16 v24, v58, s96, v24\n\tv_dot2_f32_bf16 v25, v58, s87, v25\n\tv_dot2_f32_bf16 v26, v59, s96, v26\n\tv_dot2_f32_bf16 v27, v59, s87, v27\n\tv_permlane32_swap_b32 v20, v24\n\tv_permlane32_swap_b32 v21, v25\n\tv_permlane32_swap_b32 v22, v26\n\tv_permlane32_swap_b32 v23, v27\n\tv_add_f32 v20, v20, v24\n\tv_add_f32 v21, v21, v25\n\tv_add_f32 v22, v22, v26\n\tv_add_f32 v23, v23, v27\n\ts_waitcnt vmcnt(8)\n\tv_lshlrev_b32 v24, 16, v60\n\tv_and_b32 v25, s77, v60\n\tv_lshlrev_b32 v26, 16, v61\n\tv_and_b32 v27, s77, v61\n\tv_fma_f32 v20, v20, s67, v24\n\tv_fma_f32 v21, v21, s67, v25\n\tv_fma_f32 v22, v22, s67, v26\n\tv_fma_f32 v23, v23, s67, v27\n\tv_cvt_pk_bf16_f32 v62, v20, v21\n\tv_cvt_pk_bf16_f32 v63, v22, v23\n\ts_add_u32 s83, s71, 512\n\tbuffer_store_dwordx2 v[62:63], v221, s[52:55], s83 offen\n\ts_waitcnt vmcnt(8)\n\tv_dot2_f32_bf16 v20, v64, s96, 0\n\tv_dot2_f32_bf16 v21, v64, s87, 0\n\tv_dot2_f32_bf16 v22, v65, s96, 0\n\tv_dot2_f32_bf16 v23, v65, s87, 0\n\tv_dot2_f32_bf16 v24, v66, s96, 0\n\tv_dot2_f32_bf16 v25, v66, s87, 0\n\tv_dot2_f32_bf16 v26, v67, s96, 0\n\tv_dot2_f32_bf16 v27, v67, s87, 0\n\ts_waitcnt vmcnt(7)\n\tv_dot2_f32_bf16 v20, v68, s96, v20\n\tv_dot2_f32_bf16 v21, v68, s87, v21\n\tv_dot2_f32_bf16 v22, v69, s96, v22\n\tv_dot2_f32_bf16 v23, v69, s87, v23\n\tv_dot2_f32_bf16 v24, v70, s96, v24\n\tv_dot2_f32_bf16 v25, v70, s87, v25\n\tv_dot2_f32_bf16 v26, v71, s96, v26\n\tv_dot2_f32_bf16 v27, v71, s87, v27\n\ts_waitcnt vmcnt(6)\n\tv_dot2_f32_bf16 v20, v72, s96, v20\n\tv_dot2_f32_bf16 v21, v72, s87, v21\n\tv_dot2_f32_bf16 v22, v73, s96, v22\n\tv_dot2_f32_bf16 v23, v73, s87, v23\n\tv_dot2_f32_bf16 v24, v74, s96, v24\n\tv_dot2_f32_bf16 v25, v74, s87, v25\n\tv_dot2_f32_bf16 v26, v75, s96, v26\n\tv_dot2_f32_bf16 v27, v75, s87, v27\n\ts_waitcnt vmcnt(5)\n\tv_dot2_f32_bf16 v20, v76, s96, v20\n\tv_dot2_f32_bf16 v21, v76, s87, v21\n\tv_dot2_f32_bf16 v22, v77, s96, v22\n\tv_dot2_f32_bf16 v23, v77, s87, v23\n\tv_dot2_f32_bf16 v24, v78, s96, v24\n\tv_dot2_f32_bf16 v25, v78, s87, v25\n\tv_dot2_f32_bf16 v26, v79, s96, v26\n\tv_dot2_f32_bf16 v27, v79, s87, v27\n\tv_permlane32_swap_b32 v20, v24\n\tv_permlane32_swap_b32 v21, v25\n\tv_permlane32_swap_b32 v22, v26\n\tv_permlane32_swap_b32 v23, v27\n\tv_add_f32 v20, v20, v24\n\tv_add_f32 v21, v21, v25\n\tv_add_f32 v22, v22, v26\n\tv_add_f32 v23, v23, v27\n\ts_waitcnt vmcnt(4)\n\tv_lshlrev_b32 v24, 16, v80\n\tv_and_b32 v25, s77, v80\n\tv_lshlrev_b32 v26, 16, v81\n\tv_and_b32 v27, s77, v81\n\tv_fma_f32 v20, v20, s67, v24\n\tv_fma_f32 v21, v21, s67, v25\n\tv_fma_f32 v22, v22, s67, v26\n\tv_fma_f32 v23, v23, s67, v27\n\tv_cvt_pk_bf16_f32 v82, v20, v21\n\tv_cvt_pk_bf16_f32 v83, v22, v23\n\ts_add_u32 s83, s71, 768\n\tbuffer_store_dwordx2 v[82:83], v221, s[52:55], s83 offen\n\ts_waitcnt vmcnt(4)\n\tv_dot2_f32_bf16 v20, v204, s96, 0\n\tv_dot2_f32_bf16 v21, v204, s87, 0\n\tv_dot2_f32_bf16 v22, v205, s96, 0\n\tv_dot2_f32_bf16 v23, v205, s87, 0\n\tv_dot2_f32_bf16 v24, v206, s96, 0\n\tv_dot2_f32_bf16 v25, v206, s87, 0\n\tv_dot2_f32_bf16 v26, v207, s96, 0\n\tv_dot2_f32_bf16 v27, v207, s87, 0\n\ts_waitcnt vmcnt(3)\n\tv_dot2_f32_bf16 v20, v208, s96, v20\n\tv_dot2_f32_bf16 v21, v208, s87, v21\n\tv_dot2_f32_bf16 v22, v209, s96, v22\n\tv_dot2_f32_bf16 v23, v209, s87, v23\n\tv_dot2_f32_bf16 v24, v210, s96, v24\n\tv_dot2_f32_bf16 v25, v210, s87, v25\n\tv_dot2_f32_bf16 v26, v211, s96, v26\n\tv_dot2_f32_bf16 v27, v211, s87, v27\n\tv_mov_b32 v204, v20\n\tv_mov_b32 v205, v21\n\tv_mov_b32 v206, v22\n\tv_mov_b32 v207, v23\n\tv_mov_b32 v208, v24\n\tv_mov_b32 v209, v25\n\tv_mov_b32 v210, v26\n\tv_mov_b32 v211, v27\n\tv_permlane16_swap_b32 v20, v204\n\tv_permlane16_swap_b32 v21, v205\n\tv_permlane16_swap_b32 v22, v206\n\tv_permlane16_swap_b32 v23, v207\n\tv_permlane16_swap_b32 v24, v208\n\tv_permlane16_swap_b32 v25, v209\n\tv_permlane16_swap_b32 v26, v210\n\tv_permlane16_swap_b32 v27, v211\n\tv_add_f32 v20, v20, v204\n\tv_add_f32 v21, v21, v205\n\tv_add_f32 v22, v22, v206\n\tv_add_f32 v23, v23, v207\n\tv_add_f32 v24, v24, v208\n\tv_add_f32 v25, v25, v209\n\tv_add_f32 v26, v26, v210\n\tv_add_f32 v27, v27, v211\n\tv_permlane32_swap_b32 v20, v24\n\tv_permlane32_swap_b32 v21, v25\n\tv_permlane32_swap_b32 v22, v26\n\tv_permlane32_swap_b32 v23, v27\n\tv_add_f32 v20, v20, v24\n\tv_add_f32 v21, v21, v25\n\tv_add_f32 v22, v22, v26\n\tv_add_f32 v23, v23, v27\n\ts_waitcnt vmcnt(2)\n\tv_lshlrev_b32 v24, 16, v28\n\tv_and_b32 v25, s77, v28\n\tv_lshlrev_b32 v26, 16, v29\n\tv_and_b32 v27, s77, v29\n\tv_fma_f32 v20, v20, s67, v24\n\tv_fma_f32 v21, v21, s67, v25\n\tv_fma_f32 v22, v22, s67, v26\n\tv_fma_f32 v23, v23, s67, v27\n\tv_cvt_pk_bf16_f32 v30, v20, v21\n\tv_cvt_pk_bf16_f32 v31, v22, v23\n\ts_add_u32 s83, s71, 0\n\tbuffer_store_dwordx2 v[30:31], v34, s[52:55], s83 offen\n\ts_cmp_eq_u32 s97, 0\n\ts_cbranch_scc0 L_fin_%=\n\ts_mov_b32 s83, 24133632\n\tv_mov_b32 v28, s88\n\tv_mov_b32 v29, s89\n\tbuffer_store_dwordx2 v[28:29], v42, s[56:59], s83 offen sc1\n\tL_fin_%=:\n\ts_waitcnt vmcnt(0)\n\tL_end_%=:"
    :: [x] "s"(x), [wr] "s"(wr), [wi] "s"(wi), [w1] "s"(w1), [b1] "s"(b1), [w2] "s"(w2), [out] "s"(out), [ws] "s"(ws), [tbl] "s"(tbl), [bid] "s"(bid), [wv] "s"(wv)
    : "v0", "v1", "v2", "v3", "v4", "v5", "v6", "v7", "v8", "v9", "v10", "v11", "v12", "v13", "v14", "v15", "v16", "v17", "v18", "v19", "v20", "v21", "v22", "v23", "v24", "v25", "v26", "v27", "v28", "v29", "v30", "v31", "v32", "v33", "v34", "v35", "v36", "v37", "v38", "v39", "v40", "v41", "v42", "v43", "v44", "v45", "v46", "v47", "v48", "v49", "v50", "v51", "v52", "v53", "v54", "v55", "v56", "v57", "v58", "v59", "v60", "v61", "v62", "v63", "v64", "v65", "v66", "v67", "v68", "v69", "v70", "v71", "v72", "v73", "v74", "v75", "v76", "v77", "v78", "v79", "v80", "v81", "v82", "v83", "v84", "v85", "v86", "v87", "v88", "v89", "v90", "v91", "v92", "v93", "v94", "v95", "v96", "v97", "v98", "v99", "v100", "v101", "v102", "v103", "v104", "v105", "v106", "v107", "v108", "v109", "v110", "v111", "v112", "v113", "v114", "v115", "v116", "v117", "v118", "v119", "v120", "v121", "v122", "v123", "v124", "v125", "v126", "v127", "v128", "v129", "v130", "v131", "v132", "v133", "v134", "v135", "v136", "v137", "v138", "v139", "v140", "v141", "v142", "v143", "v144", "v145", "v146", "v147", "v148", "v149", "v150", "v151", "v152", "v153", "v154", "v155", "v156", "v157", "v158", "v159", "v160", "v161", "v162", "v163", "v164", "v165", "v166", "v167", "v168", "v169", "v170", "v171", "v172", "v173", "v174", "v175", "v176", "v177", "v178", "v179", "v180", "v181", "v182", "v183", "v184", "v185", "v186", "v187", "v188", "v189", "v190", "v191", "v192", "v193", "v194", "v195", "v196", "v197", "v198", "v199", "v200", "v201", "v202", "v203", "v204", "v205", "v206", "v207", "v208", "v209", "v210", "v211", "v212", "v213", "v214", "v215", "v216", "v217", "v218", "v219", "v220", "v221", "v222", "v223", "v224", "v225", "v226", "v227", "v228", "v229", "v230", "v231", "v232", "v233", "v234", "v235", "v236", "v237", "v238", "v239", "v240", "v241", "v242", "v243", "v244", "v245", "v246", "v247", "v248", "v249", "v250", "v251", "v252", "v253", "v254", "v255", "a0", "a1", "a2", "a3", "a4", "a5", "a6", "a7", "a8", "a9", "a10", "a11", "a12", "a13", "a14", "a15", "a16", "a17", "a18", "a19", "a20", "a21", "a22", "a23", "a24", "a25", "a26", "a27", "a28", "a29", "a30", "a31", "a32", "a33", "a34", "a35", "a36", "a37", "a38", "a39", "a40", "a41", "a42", "a43", "a44", "a45", "a46", "a47", "a48", "a49", "a50", "a51", "a52", "a53", "a54", "a55", "a56", "a57", "a58", "a59", "a60", "a61", "a62", "a63", "a64", "a65", "a66", "a67", "a68", "a69", "a70", "a71", "a72", "a73", "a74", "a75", "a76", "a77", "a78", "a79", "a80", "a81", "a82", "a83", "a84", "a85", "a86", "a87", "a88", "a89", "a90", "a91", "a92", "a93", "a94", "a95", "a96", "a97", "a98", "a99", "a100", "a101", "a102", "a103", "a104", "a105", "a106", "a107", "a108", "a109", "a110", "a111", "a112", "a113", "a114", "a115", "a116", "a117", "a118", "a119", "a120", "a121", "a122", "a123", "a124", "a125", "a126", "a127", "a128", "a129", "a130", "a131", "a132", "a133", "a134", "a135", "a136", "a137", "a138", "a139", "a140", "a141", "a142", "a143", "a144", "a145", "a146", "a147", "a148", "a149", "a150", "a151", "a152", "a153", "a154", "a155", "a156", "a157", "a158", "a159", "a160", "a161", "a162", "a163", "a164", "a165", "a166", "a167", "a168", "a169", "a170", "a171", "a172", "a173", "a174", "a175", "a176", "a177", "a178", "a179", "a180", "a181", "a182", "a183", "a184", "a185", "a186", "a187", "a188", "a189", "a190", "a191", "a192", "a193", "a194", "a195", "a196", "a197", "a198", "a199", "a200", "a201", "a202", "a203", "a204", "a205", "a206", "a207", "a208", "a209", "a210", "a211", "a212", "a213", "a214", "a215", "a216", "a217", "a218", "a219", "a220", "a221", "a222", "a223", "a224", "a225", "a226", "a227", "a228", "a229", "a230", "a231", "a232", "a233", "a234", "a235", "a236", "a237", "a238", "a239", "a240", "a241", "a242", "a243", "a244", "a245", "a246", "a247", "a248", "a249", "a250", "a251", "a252", "a253", "a254", "a255", "s24", "s25", "s26", "s27", "s28", "s29", "s30", "s31", "s36", "s37", "s38", "s39", "s40", "s41", "s42", "s43", "s44", "s45", "s46", "s47", "s48", "s49", "s50", "s51", "s52", "s53", "s54", "s55", "s56", "s57", "s58", "s59", "s60", "s61", "s62", "s63", "s64", "s65", "s66", "s67", "s68", "s69", "s70", "s71", "s72", "s73", "s74", "s75", "s76", "s77", "s78", "s79", "s80", "s81", "s82", "s83", "s84", "s85", "s86", "s87", "s88", "s89", "s90", "s91", "s92", "s93", "s94", "s95", "s96", "s97", "s98", "s99", "memory", "scc", "vcc");
#endif
}

extern "C" void kernel_launch(void* const* d_in, const int* in_sizes, int n_in,
                              void* d_out, int out_size, void* d_ws, size_t ws_size,
                              hipStream_t stream) {
  fused_kernel<<<256, 256, 0, stream>>>(d_in[0], d_in[1], d_in[2], d_in[4], d_in[5], d_in[6], d_out, d_ws);
}
```
